```python
import torch

def _softmax(x, axis=-1):
    return torch.softmax(x, dim=axis)

def _gelu(x):
    return torch.nn.functional.gelu(x, approximate='tanh')

def _silu(x):
    return torch.nn.functional.silu(x)

def _sigmoid(x):
    return torch.sigmoid(x)

def _layernorm(x, axis=-1, eps=1e-05):
    m = x.mean(dim=axis, keepdim=True)
    v = x.var(dim=axis, keepdim=True, unbiased=False)
    return (x - m) / torch.sqrt(v + eps)

def _scatter_add(tgt, idx, src, axis=0):
    return tgt.clone().index_add_(axis, idx, src)

def _segment_sum(src, seg, n):
    out = torch.zeros((n,) + src.shape[1:], dtype=src.dtype)
    return out.index_add_(0, seg, src)

def _pad(x, pw):
    flat = tuple((v for pair in reversed(pw) for v in pair))
    return torch.nn.functional.pad(x, flat)
kernel_name = 'concat_heads__sliding_block__memory_aug_recurrent_mem_tr__prompt'

def setup_inputs():

    _w = lambda a: (lambda t: t.to(torch.bfloat16) if t.is_floating_point() else t)(torch.as_tensor(a))

    def _A_setup():
        B, S, D = (1, 2048, 512)
        return {'x': _w((lambda *s: torch.randn(*s, dtype=torch.bfloat16) * 0.2)(B, S, D)), 'wqkv': _w((lambda *s: torch.randn(*s, dtype=torch.bfloat16) * 0.2)(D, 3 * D)), 'wo': _w((lambda *s: torch.randn(*s, dtype=torch.bfloat16) * 0.05)(D, D))}
    _A_in = _A_setup()

    def _B_setup():
        B, S, Dm = (1, 2048, 512)
        H, Dh = (32, 128)
        M, seg = (8, 32)
        seg = min(seg, S)
        S = S // seg * seg
        return {'x': _w((lambda *s: torch.randn(*s, dtype=torch.bfloat16) * 0.05)(B, S, Dm)), 'mem0': _w((lambda *s: torch.randn(*s, dtype=torch.bfloat16) * 1.6)(B, M, Dm)), 'Wq': _w((lambda *s: torch.randn(*s, dtype=torch.bfloat16) * 0.8)(Dm, H, Dh) * Dm ** (-0.5)), 'Wk': _w((lambda *s: torch.randn(*s, dtype=torch.bfloat16) * 0.8)(Dm, H, Dh) * Dm ** (-0.5)), 'Wv': _w((lambda *s: torch.randn(*s, dtype=torch.bfloat16) * 1.6)(Dm, H, Dh) * Dm ** (-0.5)), 'Wo': _w((lambda *s: torch.randn(*s, dtype=torch.bfloat16) * 1.6)(H, Dh, Dm) * (H * Dh) ** (-0.5)), 'seg_len': seg}
    _B_in = _B_setup()
    return {'A_x': _A_in['x'], 'A_wqkv': _A_in['wqkv'], 'A_wo': _A_in['wo'], 'B_x': _B_in['x'], 'B_mem0': _B_in['mem0'], 'B_Wq': _B_in['Wq'], 'B_Wk': _B_in['Wk'], 'B_Wv': _B_in['Wv'], 'B_Wo': _B_in['Wo'], 'B_seg_len': _B_in['seg_len']}

def reference(A_x, A_wqkv, A_wo, B_x, B_mem0, B_Wq, B_Wk, B_Wv, B_Wo, B_seg_len):

    def _A_ref(x, wqkv, wo):
        B, S, D = x.shape
        H = 32
        Dh = D // H
        W = 256
        qkv = (lambda x, *d: x.permute(*d))((x @ wqkv).reshape(B, S, 3, H, Dh), 2, 0, 3, 1, 4)
        q, k, v = (qkv[0], qkv[1], qkv[2])
        scores = torch.einsum('bhqd,bhkd->bhqk', q, k) * Dh ** (-0.5)
        idx = torch.arange(S)
        mask = (idx[:, None] - idx[None, :] >= 0) & (idx[:, None] - idx[None, :] < W)
        scores = torch.where(mask[None, None], scores, -1000000000.0)
        p = _softmax(scores, axis=-1)
        o = torch.einsum('bhqk,bhkd->bhqd', p, v)
        return (lambda x, *d: x.permute(*d))(o, 0, 2, 1, 3).reshape(B, S, D) @ wo

    def _B_ref(x, mem0, Wq, Wk, Wv, Wo, seg_len):
        B, S, Dm = x.shape
        H, Dh = (Wq.shape[1], Wq.shape[2])
        M = mem0.shape[1]
        scale = Dh ** (-0.5)
        n_seg = S // seg_len
        L = M + seg_len
        idx = torch.arange(L)
        mask = (idx[:, None] < M) | (idx[:, None] >= idx[None, :])
        mem = mem0
        outs = []
        for s in range(n_seg):
            xs = x[:, s * seg_len:(s + 1) * seg_len, :].clone()
            h = torch.concatenate([mem, xs], axis=1)
            q = torch.einsum('btd,dhe->bhte', h, Wq)
            k = torch.einsum('btd,dhe->bhte', h, Wk)
            v = torch.einsum('btd,dhe->bhte', h, Wv)
            scores = torch.einsum('bhqe,bhke->bhqk', q, k) * scale
            scores = torch.where(mask[None, None], scores, -1000000000.0)
            p = _softmax(scores, axis=-1)
            o = torch.einsum('bhqk,bhke->bhqe', p, v)
            o = torch.einsum('bhte,hed->btd', o, Wo)
            mem = o[:, :M, :].clone()
            outs.append(o[:, M:, :])
        return torch.concatenate(outs, axis=1)
    a = _A_ref(A_x, A_wqkv, A_wo)
    b = _B_ref(B_x, B_mem0, B_Wq, B_Wk, B_Wv, B_Wo, B_seg_len)
    return torch.concatenate([a, b], axis=-1)
```

```cpp
#include <hip/hip_runtime.h>
#include <hip/hip_bf16.h>
#include <stdint.h>

typedef __hip_bfloat16 bf16;
__device__ __forceinline__ float b2f(bf16 x) { return __bfloat162float(x); }
__device__ __forceinline__ bf16 f2b(float x) { return __float2bfloat16(x); }

__global__ void gemm_naive(const bf16* A, int lda, const bf16* B, int ldb, bf16* C, int ldc, int M, int N, int K) {
    __shared__ float As[16][17], Bs[16][17];
    const int tx = threadIdx.x & 15, ty = threadIdx.x >> 4;
    const int row = blockIdx.y * 16 + ty, col = blockIdx.x * 16 + tx;
    float acc = 0;
    for (int k0 = 0; k0 < K; k0 += 16) {
        As[ty][tx] = (row < M) ? b2f(A[(size_t)row * lda + k0 + tx]) : 0.f;
        Bs[ty][tx] = (col < N) ? b2f(B[(size_t)(k0 + ty) * ldb + col]) : 0.f;
        __syncthreads();
        for (int k = 0; k < 16; ++k) acc += As[ty][k] * Bs[k][tx];
        __syncthreads();
    }
    if (row < M && col < N) C[(size_t)row * ldc + col] = f2b(acc);
}

__global__ void attnA(const bf16* qkv, bf16* o) {
    const int idx = blockIdx.x * blockDim.x + threadIdx.x;
    const int h = idx & 31, t = idx >> 5;
    float q[16];
    for (int d = 0; d < 16; ++d) q[d] = b2f(qkv[(size_t)t * 1536 + h * 16 + d]);
    float m = -1e30f, l = 0, acc[16];
    for (int d = 0; d < 16; ++d) acc[d] = 0;
    const int j0 = t - 255 < 0 ? 0 : t - 255;
    for (int j = j0; j <= t; ++j) {
        float s = 0;
        for (int d = 0; d < 16; ++d) s += q[d] * b2f(qkv[(size_t)j * 1536 + 512 + h * 16 + d]);
        s *= 0.25f;
        const float mn = fmaxf(m, s), a = __expf(m - mn), p = __expf(s - mn);
        l = l * a + p;
        for (int d = 0; d < 16; ++d) acc[d] = acc[d] * a + p * b2f(qkv[(size_t)j * 1536 + 1024 + h * 16 + d]);
        m = mn;
    }
    for (int d = 0; d < 16; ++d) o[(size_t)t * 512 + h * 16 + d] = f2b(acc[d] / l);
}

__global__ void attnB(const bf16* qm, const bf16* km, const bf16* vm, const bf16* qx, const bf16* kx, const bf16* vx, bf16* o  ) {
    const int h = blockIdx.x, r = blockIdx.y;
    const int e = threadIdx.x;
    __shared__ float sc[40], red[128];
    const bf16* q = r < 8 ? qm + (size_t)r * 4096 : qx + (size_t)(r - 8) * 4096;
    const float qv = b2f(q[h * 128 + e]);
    for (int j = 0; j < 40; ++j) {
        const bf16* k = j < 8 ? km + (size_t)j * 4096 : kx + (size_t)(j - 8) * 4096;
        red[e] = qv * b2f(k[h * 128 + e]);
        __syncthreads();
        for (int s = 64; s > 0; s >>= 1) { if (e < s) red[e] += red[e + s]; __syncthreads(); }
        if (e == 0) sc[j] = red[0] * 0.08838834764831845f;
        __syncthreads();
    }
    float m = -1e30f;
    for (int j = 0; j < 40; ++j) if (r < 8 || r >= j) m = fmaxf(m, sc[j]);
    float l = 0, acc = 0;
    for (int j = 0; j < 40; ++j) if (r < 8 || r >= j) {
        const float p = __expf(sc[j] - m); l += p;
        const bf16* v = j < 8 ? vm + (size_t)j * 4096 : vx + (size_t)(j - 8) * 4096;
        acc += p * b2f(v[h * 128 + e]);
    }
    o[(size_t)r * 4096 + h * 128 + e] = f2b(acc / l);
}
__global__ void scatterB(const bf16* res  , bf16* mem, bf16* out, int s) {
    const int i = blockIdx.x * blockDim.x + threadIdx.x;
    if (i >= 40 * 512) return;
    const int r = i / 512, c = i % 512;
    if (r < 8) mem[r * 512 + c] = res[i]; else out[(size_t)(s * 32 + r - 8) * 1024 + 512 + c] = res[i];
}

extern "C" void kernel_launch(void* const* d_in, const int* in_sizes, int n_in, void* d_out, int out_size, void* d_ws, size_t ws_size, hipStream_t stream) {
    const bf16* Ax = (const bf16*)d_in[0]; const bf16* Awqkv = (const bf16*)d_in[1]; const bf16* Awo = (const bf16*)d_in[2];
    const bf16* Bx = (const bf16*)d_in[3]; const bf16* mem0 = (const bf16*)d_in[4];
    const bf16* Wq = (const bf16*)d_in[5]; const bf16* Wk = (const bf16*)d_in[6]; const bf16* Wv = (const bf16*)d_in[7]; const bf16* Wo = (const bf16*)d_in[8];
    bf16* out = (bf16*)d_out;
    bf16* ws = (bf16*)d_ws;
    bf16* qkv = ws; ws += (size_t)2048 * 1536;
    bf16* oa = ws; ws += (size_t)2048 * 512;
    bf16* qx = ws; ws += (size_t)2048 * 4096; bf16* kx = ws; ws += (size_t)2048 * 4096; bf16* vx = ws; ws += (size_t)2048 * 4096;
    bf16* qm = ws; ws += 8 * 4096; bf16* km = ws; ws += 8 * 4096; bf16* vm = ws; ws += 8 * 4096;
    bf16* ob = ws; ws += 40 * 4096; bf16* res = ws; ws += 40 * 512; bf16* mem = ws; ws += 8 * 512;
    gemm_naive<<<dim3(1536 / 16, 2048 / 16), 256, 0, stream>>>(Ax, 512, Awqkv, 1536, qkv, 1536, 2048, 1536, 512);
    attnA<<<2048 * 32 / 256, 256, 0, stream>>>(qkv, oa);
    gemm_naive<<<dim3(512 / 16, 2048 / 16), 256, 0, stream>>>(oa, 512, Awo, 512, out, 1024, 2048, 512, 512);
    gemm_naive<<<dim3(4096 / 16, 2048 / 16), 256, 0, stream>>>(Bx, 512, Wq, 4096, qx, 4096, 2048, 4096, 512);
    gemm_naive<<<dim3(4096 / 16, 2048 / 16), 256, 0, stream>>>(Bx, 512, Wk, 4096, kx, 4096, 2048, 4096, 512);
    gemm_naive<<<dim3(4096 / 16, 2048 / 16), 256, 0, stream>>>(Bx, 512, Wv, 4096, vx, 4096, 2048, 4096, 512);
    hipMemcpyAsync(mem, mem0, 8 * 512 * 2, hipMemcpyDeviceToDevice, stream);
    for (int s = 0; s < 64; ++s) {
        gemm_naive<<<dim3(4096 / 16, 1), 256, 0, stream>>>(mem, 512, Wq, 4096, qm, 4096, 8, 4096, 512);
        gemm_naive<<<dim3(4096 / 16, 1), 256, 0, stream>>>(mem, 512, Wk, 4096, km, 4096, 8, 4096, 512);
        gemm_naive<<<dim3(4096 / 16, 1), 256, 0, stream>>>(mem, 512, Wv, 4096, vm, 4096, 8, 4096, 512);
        attnB<<<dim3(32, 40), 128, 0, stream>>>(qm, km, vm, qx + (size_t)s * 32 * 4096, kx + (size_t)s * 32 * 4096, vx + (size_t)s * 32 * 4096, ob);
        gemm_naive<<<dim3(512 / 16, 3), 256, 0, stream>>>(ob, 4096, Wo, 512, res, 512, 40, 512, 4096);
        scatterB<<<80, 256, 0, stream>>>(res, mem, out, s);
    }
}
```

```cpp
#include <hip/hip_runtime.h>
#include <hip/hip_bf16.h>
#include <stdint.h>

typedef __hip_bfloat16 bf16;
typedef __bf16 bf16x8 __attribute__((ext_vector_type(8)));
typedef __bf16 bf16x4 __attribute__((ext_vector_type(4)));
typedef __bf16 bf16x2 __attribute__((ext_vector_type(2)));
typedef __bf16 bf16x16 __attribute__((ext_vector_type(16)));
typedef float f32x4 __attribute__((ext_vector_type(4)));
typedef float f32x2 __attribute__((ext_vector_type(2)));
typedef unsigned u32x4 __attribute__((ext_vector_type(4)));
typedef unsigned u32x2 __attribute__((ext_vector_type(2)));
typedef unsigned u32x8 __attribute__((ext_vector_type(8)));
#define LAS __attribute__((address_space(3)))
#define GAS __attribute__((address_space(1)))
typedef GAS unsigned gu32;
#define RLX_AGENT __ATOMIC_RELAXED, __HIP_MEMORY_SCOPE_AGENT

__device__ __forceinline__ float b2f(bf16 x) { return __bfloat162float(x); }
__device__ __forceinline__ bf16 f2b(float x) { return __float2bfloat16(x); }

#define FSTRIDE 32
#define PSLEEP 2
#define KVRING 64
constexpr int RING = 4;
constexpr int NVREG = 6;
constexpr size_t WS_MEMS = 0;
constexpr size_t WS_PART = WS_MEMS + 64 * 8192;
constexpr size_t WS_FLAG = WS_PART + (size_t)RING * 64 * 8192;
constexpr size_t FLAG_BYTES = 3 * 2048 * FSTRIDE * 4 + 32768;
constexpr size_t WS_INIT_BYTES = WS_FLAG + FLAG_BYTES;
constexpr size_t WS_KX = WS_INIT_BYTES;
constexpr int KVR = KVRING;
constexpr size_t WS_VX = WS_KX + (size_t)KVR * 32 * 8192;
constexpr size_t WS_KM = WS_VX + (size_t)KVR * 32 * 8192;
constexpr size_t WS_VM = WS_KM + (size_t)KVR * 32 * 2048;
constexpr size_t WS_OX = WS_VM + (size_t)KVR * 32 * 2048;
constexpr size_t WS_AQ = WS_OX + (size_t)64 * 2 * 128 * 1024;
constexpr size_t WS_AO = WS_AQ + (size_t)3 * 32 * 128 * 512;
constexpr size_t WS_END1 = WS_AO + (size_t)128 * 16 * 1024;
constexpr int FS = FSTRIDE;
constexpr int FL_KV = 0;
constexpr int FL_KM = FL_KV + 2048 * FS;
constexpr int FL_VM = FL_KM + 2048 * FS;
constexpr int FL_O = FL_VM + 2048 * FS;
constexpr int FL_A1 = FL_O + 64 * 32;
constexpr int FL_A2 = FL_A1 + 16 * 32;
constexpr int FL_TMO = FL_A2 + 32 * 32;

constexpr unsigned SPIN_CAP = 1u << 20;

constexpr unsigned L_MEM = 0;
constexpr unsigned L_KX = 8192;
constexpr unsigned L_VX = L_KX + 2 * 12288;
constexpr unsigned L_Q = L_VX + 2 * 8192;
constexpr unsigned L_O = L_Q + 4096;
constexpr unsigned L_VM = L_O + 2048;
constexpr unsigned L_FLAG = L_VM + 2048;
constexpr unsigned L_DUMMY = L_FLAG + 64;
constexpr unsigned L_WV = 65536;
constexpr unsigned L_WO = 98304;
constexpr unsigned L_STG = 0;
constexpr unsigned LDS_BYTES = 160 * 1024;

__device__ __forceinline__ u32x4 lds_rd16(unsigned a) { return *(LAS u32x4*)(size_t)a; }
__device__ __forceinline__ void lds_wr16(unsigned a, u32x4 v) { *(LAS u32x4*)(size_t)a = v; }
__device__ __forceinline__ void lds_wr4(unsigned a, unsigned v) { *(LAS unsigned*)(size_t)a = v; }
__device__ __forceinline__ void lds_wr8(unsigned a, u32x2 v) { *(LAS u32x2*)(size_t)a = v; }
__device__ __forceinline__ unsigned lds_rd4(unsigned a) { return *(LAS unsigned*)(size_t)a; }
__device__ __forceinline__ u32x2 lds_rd8(unsigned a) { return *(LAS u32x2*)(size_t)a; }
__device__ __forceinline__ void wg_barrier() { asm volatile("s_waitcnt lgkmcnt(0)" ::: "memory"); __builtin_amdgcn_s_barrier(); asm volatile("" ::: "memory"); }
__device__ __forceinline__ unsigned pk2(float a, float b) { bf16x2 v = {(__bf16)a, (__bf16)b}; return __builtin_bit_cast(unsigned, v); }
__device__ __forceinline__ unsigned max4(u32x4 a) { return max(max(a.x, a.y), max(a.z, a.w)); }
__device__ __forceinline__ void dma16(const __amdgpu_buffer_rsrc_t r, unsigned voff, unsigned lds_dst) {
    unsigned keep;
    asm volatile("s_mov_b32 %0, m0\n\ts_mov_b32 m0, %3\n\ts_nop 0\n\tbuffer_load_dwordx4 %1, %2, 0 offen sc1 lds\n\ts_mov_b32 m0, %0" : "=&s"(keep) : "v"(voff), "s"(r), "s"(lds_dst) : "memory");
}
__device__ __forceinline__ float xor8f(float v) { return __builtin_bit_cast(float, __builtin_amdgcn_update_dpp(0, __builtin_bit_cast(int, v), 0x128  , 0xf, 0xf, false)); }
__device__ __forceinline__ float xor16f(float v) {
    auto r = __builtin_amdgcn_permlane16_swap(__builtin_bit_cast(unsigned, v), __builtin_bit_cast(unsigned, v), false, false);
    return __builtin_bit_cast(float, (threadIdx.x & 16) ? r[0] : r[1]);
}
__device__ __forceinline__ float xor32f(float v) {
    auto r = __builtin_amdgcn_permlane32_swap(__builtin_bit_cast(unsigned, v), __builtin_bit_cast(unsigned, v), false, false);
    return __builtin_bit_cast(float, (threadIdx.x & 32) ? r[0] : r[1]);
}

__device__ __forceinline__ void tr8x8(const u32x4 (&r)[8], u32x4 (&f)[8]) {
#pragma unroll
    for (int t = 0; t < 8; ++t) {
        const unsigned sel = (t & 1) ? 0x07060302u : 0x05040100u;
#pragma unroll
        for (int d = 0; d < 4; ++d) f[t][d] = __builtin_amdgcn_perm(r[2 * d + 1][t >> 1], r[2 * d][t >> 1], sel);
    }
}

#define SMF_A(acc, A_, W_) asm volatile("v_smfmac_f32_16x16x64_bf16 %0, %1, %2, %3" : "+v"(acc) : "v"(A_), "a"(W_), "v"(idx))
#define SMF_V(acc, A_, W_) asm volatile("v_smfmac_f32_16x16x64_bf16 %0, %1, %2, %3" : "+v"(acc) : "v"(A_), "v"(W_), "v"(idx))
#define SMF_VN(acc, A_, W_) asm volatile("s_nop 1\n\tv_smfmac_f32_16x16x64_bf16 %0, %1, %2, %3" : "+v"(acc) : "v"(A_), "v"(W_), "v"(idx))
__device__ __forceinline__ float vadd(float a, float b) { float r; asm("v_add_f32 %0, %1, %2" : "=v"(r) : "v"(a), "v"(b)); return r; }
__device__ __forceinline__ float vmax3(float a, float b, float c) { float r; asm("v_max3_f32 %0, %1, %2, %3" : "=v"(r) : "v"(a), "v"(b), "v"(c)); return r; }
__device__ __forceinline__ float vmax2(float a, float b) { float r; asm("v_max_f32 %0, %1, %2" : "=v"(r) : "v"(a), "v"(b)); return r; }
__device__ __forceinline__ unsigned vnosent(float a) { unsigned r; asm("v_min_u32 %0, %1, %2" : "=v"(r) : "v"(a), "v"(0xFFFFFFFEu)); return r; }
__device__ __forceinline__ void swap16(float& a, float& b) { asm volatile("s_nop 1\n\tv_permlane16_swap_b32 %0, %1" : "+v"(a), "+v"(b)); }
__device__ __forceinline__ void swap32(float& a, float& b) { asm volatile("s_nop 1\n\tv_permlane32_swap_b32 %0, %1" : "+v"(a), "+v"(b)); }
__device__ __forceinline__ void pack_rows(const f32x4 a, const f32x4 b, unsigned& r0, unsigned& r1) {
    float t0, t1, t2, t3;
    asm("v_add_f32 %2, %6, %7\n\tv_add_f32 %3, %10, %11\n\tv_add_f32 %4, %8, %9\n\tv_add_f32 %5, %12, %13\n\t"
        "v_cvt_pk_bf16_f32 %0, %2, %3\n\tv_cvt_pk_bf16_f32 %1, %4, %5"
        : "=&v"(r0), "=&v"(r1), "=&v"(t0), "=&v"(t1), "=&v"(t2), "=&v"(t3)
        : "v"(a[0]), "v"(a[1]), "v"(a[2]), "v"(a[3]), "v"(b[0]), "v"(b[1]), "v"(b[2]), "v"(b[3]));
}
__device__ __forceinline__ void pack_cols(const f32x4 a, const f32x4 b, unsigned& r0, unsigned& r1) {
    float t0, t1, t2, t3;
    asm("v_add_f32 %2, %6, %7\n\tv_add_f32 %3, %8, %9\n\tv_add_f32 %4, %10, %11\n\tv_add_f32 %5, %12, %13\n\t"
        "v_cvt_pk_bf16_f32 %0, %2, %3\n\tv_cvt_pk_bf16_f32 %1, %4, %5"
        : "=&v"(r0), "=&v"(r1), "=&v"(t0), "=&v"(t1), "=&v"(t2), "=&v"(t3)
        : "v"(a[0]), "v"(a[1]), "v"(a[2]), "v"(a[3]), "v"(b[0]), "v"(b[1]), "v"(b[2]), "v"(b[3]));
}
__device__ __forceinline__ float neg_max_scaled(const f32x4 S0, const f32x4 S1, const f32x4 S2, float sc) {
    float m, t;
    asm("v_max3_f32 %0, %2, %3, %4\n\tv_max3_f32 %1, %5, %6, %7\n\tv_max3_f32 %0, %0, %8, %9\n\tv_max3_f32 %1, %1, %10, %11\n\tv_max_f32 %0, %0, %1\n\t"
        "v_mov_b32 %1, %0\n\ts_nop 1\n\tv_permlane16_swap_b32 %0, %1\n\tv_max_f32 %0, %0, %1\n\t"
        "v_mov_b32 %1, %0\n\ts_nop 1\n\tv_permlane32_swap_b32 %0, %1\n\tv_max_f32 %0, %0, %1\n\tv_mul_f32 %0, %0, %12"
        : "=&v"(m), "=&v"(t)
        : "v"(S1[0]), "v"(S1[1]), "v"(S1[2]), "v"(S1[3]), "v"(S2[0]), "v"(S2[1]), "v"(S0[0]), "v"(S0[1]), "v"(S0[2]), "v"(S0[3]), "v"(sc));
    return m;
}
__device__ __forceinline__ float recip_sum(const float (&p0)[4], const float (&p1)[4], const float (&p2)[2]) {
    float a, b, c;
    asm("v_add_f32 %0, %3, %4\n\tv_add_f32 %1, %5, %6\n\tv_add_f32 %2, %7, %8\n\tv_add_f32 %0, %0, %9\n\tv_add_f32 %1, %1, %10\n\tv_add_f32 %2, %2, %11\n\t"
        "v_add_f32 %0, %0, %12\n\tv_add_f32 %1, %1, %2\n\tv_add_f32 %0, %0, %1\n\t"
        "v_mov_b32 %1, %0\n\ts_nop 1\n\tv_permlane16_swap_b32 %0, %1\n\tv_add_f32 %0, %0, %1\n\t"
        "v_mov_b32 %1, %0\n\ts_nop 1\n\tv_permlane32_swap_b32 %0, %1\n\tv_add_f32 %0, %0, %1\n\tv_rcp_f32 %0, %0\n\ts_nop 0"
        : "=&v"(a), "=&v"(b), "=&v"(c)
        : "v"(p0[0]), "v"(p0[1]), "v"(p0[2]), "v"(p0[3]), "v"(p1[0]), "v"(p1[1]), "v"(p1[2]), "v"(p1[3]), "v"(p2[0]), "v"(p2[1]));
    return a;
}
__device__ __forceinline__ u32x4 scale_pack(const f32x4 a, const f32x4 b, float r) {
    u32x4 o; float t0, t1;
    asm("v_mul_f32 %4, %6, %14\n\tv_mul_f32 %5, %10, %14\n\tv_cvt_pk_bf16_f32 %0, %4, %5\n\t"
        "v_mul_f32 %4, %7, %14\n\tv_mul_f32 %5, %11, %14\n\tv_cvt_pk_bf16_f32 %1, %4, %5\n\t"
        "v_mul_f32 %4, %8, %14\n\tv_mul_f32 %5, %12, %14\n\tv_cvt_pk_bf16_f32 %2, %4, %5\n\t"
        "v_mul_f32 %4, %9, %14\n\tv_mul_f32 %5, %13, %14\n\tv_cvt_pk_bf16_f32 %3, %4, %5"
        : "=&v"(o.x), "=&v"(o.y), "=&v"(o.z), "=&v"(o.w), "=&v"(t0), "=&v"(t1)
        : "v"(a[0]), "v"(a[1]), "v"(a[2]), "v"(a[3]), "v"(b[0]), "v"(b[1]), "v"(b[2]), "v"(b[3]), "v"(r));
    return o;
}
__device__ __forceinline__ u32x4 part_pack(const f32x4 a, const f32x4 b, unsigned lim) {
    u32x4 o;
    asm("v_add_f32 %0, %4, %5\n\tv_add_f32 %1, %8, %9\n\tv_add_f32 %2, %6, %7\n\tv_add_f32 %3, %10, %11\n\t"
        "v_min_u32 %0, %0, %12\n\tv_min_u32 %1, %1, %12\n\tv_min_u32 %2, %2, %12\n\tv_min_u32 %3, %3, %12"
        : "=&v"(o.x), "=&v"(o.y), "=&v"(o.z), "=&v"(o.w)
        : "v"(a[0]), "v"(a[1]), "v"(a[2]), "v"(a[3]), "v"(b[0]), "v"(b[1]), "v"(b[2]), "v"(b[3]), "s"(lim));
    return o;
}
__device__ __forceinline__ float max8_x(const f32x4 a, const f32x4 b) {
    float m, t;
    asm("v_max3_f32 %0, %2, %3, %4\n\tv_max3_f32 %1, %5, %6, %7\n\tv_max3_f32 %0, %0, %8, %9\n\tv_max_f32 %0, %0, %1\n\t"
        "v_mov_b32 %1, %0\n\ts_nop 1\n\tv_permlane16_swap_b32 %0, %1\n\tv_max_f32 %0, %0, %1\n\t"
        "v_mov_b32 %1, %0\n\ts_nop 1\n\tv_permlane32_swap_b32 %0, %1\n\tv_max_f32 %0, %0, %1"
        : "=&v"(m), "=&v"(t)
        : "v"(a[0]), "v"(a[1]), "v"(a[2]), "v"(a[3]), "v"(b[0]), "v"(b[1]), "v"(b[2]), "v"(b[3]));
    return m;
}
__device__ __forceinline__ float max2_x(float a, float b, float mx) {
    float m, t;
    asm("v_max_f32 %0, %2, %3\n\t"
        "v_mov_b32 %1, %0\n\ts_nop 1\n\tv_permlane16_swap_b32 %0, %1\n\tv_max_f32 %0, %0, %1\n\t"
        "v_mov_b32 %1, %0\n\ts_nop 1\n\tv_permlane32_swap_b32 %0, %1\n\tv_max3_f32 %0, %0, %1, %4"
        : "=&v"(m), "=&v"(t) : "v"(a), "v"(b), "v"(mx));
    return m;
}
__device__ __forceinline__ float sum8_x(const float (&p)[4], const float (&q)[4]) {
    float a, b;
    asm("v_add_f32 %0, %2, %3\n\tv_add_f32 %1, %4, %5\n\tv_add_f32 %0, %0, %6\n\tv_add_f32 %1, %1, %7\n\tv_add_f32 %0, %0, %8\n\tv_add_f32 %1, %1, %9\n\tv_add_f32 %0, %0, %1\n\t"
        "v_mov_b32 %1, %0\n\ts_nop 1\n\tv_permlane16_swap_b32 %0, %1\n\tv_add_f32 %0, %0, %1\n\t"
        "v_mov_b32 %1, %0\n\ts_nop 1\n\tv_permlane32_swap_b32 %0, %1\n\tv_add_f32 %0, %0, %1"
        : "=&v"(a), "=&v"(b)
        : "v"(p[0]), "v"(p[1]), "v"(p[2]), "v"(p[3]), "v"(q[0]), "v"(q[1]), "v"(q[2]), "v"(q[3]));
    return a;
}
__device__ __forceinline__ float recip_l(float a, float b, float al, float lx) {
    float r, t;
    asm("v_add_f32 %0, %2, %3\n\t"
        "v_mov_b32 %1, %0\n\ts_nop 1\n\tv_permlane16_swap_b32 %0, %1\n\tv_add_f32 %0, %0, %1\n\t"
        "v_mov_b32 %1, %0\n\ts_nop 1\n\tv_permlane32_swap_b32 %0, %1\n\tv_add_f32 %0, %0, %1\n\tv_fma_f32 %0, %4, %5, %0\n\tv_rcp_f32 %0, %0\n\ts_nop 0"
        : "=&v"(r), "=&v"(t) : "v"(a), "v"(b), "v"(al), "v"(lx));
    return r;
}
__device__ __forceinline__ u32x4 pack_o(const f32x4 a, const f32x4 b) {
    u32x4 o;
    asm("v_cvt_pk_bf16_f32 %0, %4, %8\n\tv_cvt_pk_bf16_f32 %1, %5, %9\n\tv_cvt_pk_bf16_f32 %2, %6, %10\n\tv_cvt_pk_bf16_f32 %3, %7, %11"
        : "=&v"(o.x), "=&v"(o.y), "=&v"(o.z), "=&v"(o.w)
        : "v"(a[0]), "v"(a[1]), "v"(a[2]), "v"(a[3]), "v"(b[0]), "v"(b[1]), "v"(b[2]), "v"(b[3]));
    return o;
}
#define MF0(acc, A_, B_) asm volatile("v_mfma_f32_16x16x32_bf16 %0, %1, %2, 0" : "=&v"(acc) : "v"(A_), "v"(B_))
#define MFA(acc, A_, B_) asm volatile("v_mfma_f32_16x16x32_bf16 %0, %1, %2, %0" : "+v"(acc) : "v"(A_), "v"(B_))
struct ChainArgs {
    const bf16* x; bf16* out; const bf16* ax; const bf16* awqkv; const bf16* awo;
    const bf16* mem0; const bf16* Wq; const bf16* Wk; const bf16* Wv; const bf16* Wo;
    unsigned char* ws;
    int dA, dB, stop, pad;
};

__device__ __forceinline__ bool wait_flag(gu32* f, gu32* tmo) {
    for (unsigned spins = 0;; ++spins) {
        const unsigned v = __builtin_amdgcn_readfirstlane(__hip_atomic_load(f, RLX_AGENT));
        if (v != 0xFFFFFFFFu) return true;
        if (spins > SPIN_CAP) { __hip_atomic_store(tmo, 1u, RLX_AGENT); return false; }
        __builtin_amdgcn_s_sleep(PSLEEP);
    }
}

__device__ __forceinline__ void chain_role(const ChainArgs& a, const int b) {
    const int h = b >> 1, nh = b & 1;
    const int tid = threadIdx.x, lane = tid & 63, w = __builtin_amdgcn_readfirstlane(tid >> 6);
    const int g = lane >> 4, c = lane & 15;
    const __amdgpu_buffer_rsrc_t ws_r = __builtin_amdgcn_make_buffer_rsrc(a.ws, 0, 0x7fffffff, 0x00020000);
    gu32* flags = (gu32*)(a.ws + WS_FLAG);
    gu32* tmo = flags + FL_TMO;

#define STAMP(k)
#define CT(k)
    u32x8 W[3][2][8];
    u32x4 pend[2 * (8 - NVREG) * 2];
    {
        const int rj[8] = {0, 1, 8, 9, 2, 3, 10, 11};
#pragma unroll
        for (int M = 0; M < 3; ++M) {
            const bf16* Wm = (M == 0 ? a.Wq : (M == 1 ? a.Wk : a.Wv)) + h * 128 + 8 * c;
#pragma unroll
            for (int it = 0; it < 4; ++it) {
                const int df = 4 * w + it;
                const int rb = 32 * df + 16 * (g >> 1) + 4 * (g & 1);
                u32x4 r[8], f[8];
#pragma unroll
                for (int j = 0; j < 8; ++j) r[j] = *(const u32x4*)(Wm + (size_t)(rb + rj[j]) * 4096);
                tr8x8(r, f);
#pragma unroll
                for (int t = 0; t < 8; ++t) lds_wr16(L_STG + (unsigned)(((t * 16 + df) * 64 + lane) * 16), f[t]);
            }
            wg_barrier();
#pragma unroll
            for (int tau = 0; tau < 2; ++tau)
#pragma unroll
                for (int kk = 0; kk < 8; ++kk) {
                    const u32x4 lo = lds_rd16(L_STG + (unsigned)((((2 * w + tau) * 16 + 2 * kk) * 64 + lane) * 16));
                    const u32x4 hi = lds_rd16(L_STG + (unsigned)((((2 * w + tau) * 16 + 2 * kk + 1) * 64 + lane) * 16));
                    u32x8 v = {lo.x, lo.y, lo.z, lo.w, hi.x, hi.y, hi.z, hi.w};
                    W[M][tau][kk] = v;
                    if (M < 2) asm volatile("" : "+a"(W[M][tau][kk]));
                    else if (kk >= NVREG) {
                        pend[(tau * (8 - NVREG) + kk - NVREG) * 2] = lo; pend[(tau * (8 - NVREG) + kk - NVREG) * 2 + 1] = hi;
                    }
                }
            wg_barrier();
        }
#pragma unroll
        for (int q2 = 0; q2 < 2 * (8 - NVREG) * 2; ++q2) lds_wr16(L_WV + (unsigned)(((w * 2 * (8 - NVREG) * 2 + q2) * 64 + lane) * 16), pend[q2]);
        const int rj2[8] = {0, 1, 2, 3, 8, 9, 10, 11};
#pragma unroll
        for (int half = 0; half < 2; ++half) {
            const int m = 16 * half + c;
            const int rb = 32 * w + 16 * (g & 1) + 4 * (g >> 1);
            const bf16* Wp = a.Wo + (size_t)h * 128 * 512 + 256 * nh + 8 * m;
            u32x4 r[8], f[8];
#pragma unroll
            for (int j = 0; j < 8; ++j) r[j] = *(const u32x4*)(Wp + (size_t)(rb + rj2[j]) * 512);
            tr8x8(r, f);
#pragma unroll
            for (int t = 0; t < 8; ++t) {
                const int n = 8 * m + t;
                const int ww = n >> 6, n6 = n & 63, G4 = n6 >> 4, n4 = n6 & 15;
                const int tp = 2 * (G4 >> 1) + (n4 & 1), cc = 8 * (G4 & 1) + (n4 >> 1);
                lds_wr16(L_WO + (unsigned)((((ww * 4 + tp) * 4 + w) * 64 + g * 16 + cc) * 16), f[t]);
            }
        }
    }
#pragma unroll
    for (int q2 = 0; q2 < 2; ++q2) {
        const int kk = 2 * w + q2;
        const u32x4 v = *(const u32x4*)(a.mem0 + (c >> 1) * 512 + 64 * kk + 16 * g + 8 * (c & 1));
        lds_wr16(L_MEM + (unsigned)((kk * 64 + lane) * 16), v);
    }
    if (w == 1 || w == 2) {
        wait_flag(flags + FL_KV + (0 * 32 + h) * FS, tmo);
        const unsigned src = (unsigned)((w == 1 ? WS_KX : WS_VX) + (size_t)(0 * 32 + h) * 8192);
        const unsigned dst = (w == 1 ? L_KX : L_VX);
#pragma unroll
        for (int k = 0; k < 8; ++k) dma16(ws_r, src + k * 1024 + lane * 16, dst + k * 1024);
        asm volatile("s_waitcnt vmcnt(0)" ::: "memory");
    }
    if (tid == 0) lds_wr4(L_FLAG, 0);
    wg_barrier();

    const int idx = (c & 1) ? 0xEEEE : 0x4444;
    const float sc2 = 0.08838834764831845f * 1.4426950408889634f;
    unsigned epos = (unsigned)(((c >> 2) * 64 + (2 * (c & 1) + (w >> 1)) * 16 + 2 * g) * 16 + (4 * ((c >> 1) & 1) + 2 * (w & 1)) * 2);
    const int gp = c >> 2, rp = c & 3;
    unsigned qwo = L_Q + epos;
    unsigned l16 = (unsigned)lane * 16;
    unsigned a0o = 8192u + (unsigned)((g * 16 + 2 * gp + (rp & 1)) * 16);
    unsigned a2o = 4096u + (unsigned)((g * 16 + 4 * gp + 2 + (rp & 1)) * 16);
    unsigned qao = L_Q + (unsigned)((g * 16 + (c & 7)) * 16);
    unsigned wvb = L_WV + (unsigned)(w * 2 * (8 - NVREG) * 2 * 1024) + l16;
    unsigned wob = L_WO + (unsigned)(w * 16384) + l16;
    unsigned owo = (c < 8) ? L_O + (unsigned)(((g >> 1) * 64 + (2 * (g & 1) + (w >> 1)) * 16 + 2 * (c & 7) + (w & 1)) * 16) : L_DUMMY + (unsigned)tid * 16;
    unsigned pvo = (unsigned)((((2 * (16 * nh + 4 * w + (c >> 3)) + (g >> 1)) * 32 + h) * 2 + (g & 1)) * 128 + (c & 7) * 16);

    asm volatile("" : "+v"(l16), "+v"(a0o), "+v"(a2o), "+v"(qao), "+v"(wvb), "+v"(wob), "+v"(owo), "+v"(pvo), "+v"(epos), "+v"(qwo));
    f32x4 aq[2], ak[2], av[2];
    u32x4 pm0 = {0u, 0u, 0u, 0u}, pm1 = pm0, vmt = pm0;
#define ZACC() do { _Pragma("unroll") for (int t = 0; t < 2; ++t) { aq[t] = (f32x4){0, 0, 0, 0}; ak[t] = aq[t]; av[t] = aq[t]; } \
        asm volatile("" : "+v"(aq[0]), "+v"(aq[1]), "+v"(ak[0]), "+v"(ak[1]), "+v"(av[0]), "+v"(av[1])); } while (0)
    ZACC();
    for (int s = 0; s < 64; ++s) {
        const unsigned kxb = L_KX + (unsigned)(s & 1) * 12288, vxb = L_VX + (unsigned)(s & 1) * 8192;
        unsigned kvflag = 1u;
        if ((w == 1 || w == 2) && s < 63) kvflag = __hip_atomic_load(flags + FL_KV + ((s + 1) * 32 + h) * FS, RLX_AGENT);
        STAMP(0);
        CT(0);
#define SB() __builtin_amdgcn_sched_barrier(0)
        u32x4 A[8];
#pragma unroll
        for (int kk = 0; kk < 8; ++kk) A[kk] = lds_rd16(L_MEM + l16 + (unsigned)(kk * 1024));
        SB();
        const unsigned kxl = kxb + l16, k0l = kxb + a0o, vxl = vxb + (unsigned)(2 * w * 1024) + l16, kml = kxb + 8192 + epos;
        u32x4 ka[4], kb[4], vx[2], qf[4], k0[4];
        u32x8 wvl[2][8 - NVREG];
#pragma unroll
        for (int kk = 0; kk < 8; ++kk) {
            SMF_A(aq[0], A[kk], W[0][0][kk]); SMF_A(aq[1], A[kk], W[0][1][kk]);
            if (kk >= 2 && kk <= 5) { ka[kk - 2] = lds_rd16(kxl + (unsigned)((kk - 2) * 1024)); kb[kk - 2] = lds_rd16(kxl + 4096 + (unsigned)((kk - 2) * 1024)); SB(); }
        }
#pragma unroll
        for (int kk = 0; kk < 3; ++kk) { SMF_A(ak[0], A[kk], W[1][0][kk]); SMF_A(ak[1], A[kk], W[1][1][kk]); }
        asm volatile("" : "+v"(aq[0]), "+v"(aq[1]));
        { unsigned r0, r1; pack_rows(aq[0], aq[1], r0, r1); lds_wr4(qwo, r0); lds_wr4(qwo + 16, r1); }
        SB();
#pragma unroll
        for (int kk = 3; kk < 5; ++kk) { SMF_A(ak[0], A[kk], W[1][0][kk]); SMF_A(ak[1], A[kk], W[1][1][kk]); }
        CT(1);
        wg_barrier();
#pragma unroll
        for (int ks = 0; ks < 4; ++ks) qf[ks] = lds_rd16(qao + (unsigned)(ks * 1024));
        vx[0] = lds_rd16(vxl); vx[1] = lds_rd16(vxl + 1024);
        SB();
#pragma unroll
        for (int kk = 5; kk < 8; ++kk) { SMF_A(ak[0], A[kk], W[1][0][kk]); SMF_A(ak[1], A[kk], W[1][1][kk]); }
#pragma unroll
        for (int kk = 0; kk < 2; ++kk) { SMF_V(av[0], A[kk], W[2][0][kk]); SMF_V(av[1], A[kk], W[2][1][kk]); }
        f32x4 Sa, Sb, Sm;
        MF0(Sa, ka[0], qf[0]); MF0(Sb, kb[0], qf[0]); MFA(Sa, ka[1], qf[1]); MFA(Sb, kb[1], qf[1]);
        asm volatile("" : "+v"(ak[0]), "+v"(ak[1]));
        { unsigned r0, r1; pack_rows(ak[0], ak[1], r0, r1); lds_wr4(kml, r0); lds_wr4(kml + 16, r1); }
        SB();
        MFA(Sa, ka[2], qf[2]); MFA(Sb, kb[2], qf[2]); MFA(Sa, ka[3], qf[3]); MFA(Sb, kb[3], qf[3]);
        CT(2);
        wg_barrier();
        CT(3);
#pragma unroll
        for (int ks = 0; ks < 4; ++ks) k0[ks] = lds_rd16(k0l + (unsigned)(ks * 1024));
#pragma unroll
        for (int t = 0; t < 2; ++t)
#pragma unroll
            for (int kk = 0; kk < 8 - NVREG; ++kk) {
                const u32x4 lo = lds_rd16(wvb + (unsigned)(((t * (8 - NVREG) + kk) * 2) * 1024));
                const u32x4 hi = lds_rd16(wvb + (unsigned)(((t * (8 - NVREG) + kk) * 2 + 1) * 1024));
                wvl[t][kk] = (u32x8){lo.x, lo.y, lo.z, lo.w, hi.x, hi.y, hi.z, hi.w};
            }
        SB();
        SMF_V(av[0], A[2], W[2][0][2]); SMF_V(av[1], A[2], W[2][1][2]);
        asm volatile("" : "+v"(Sa), "+v"(Sb));
        const float mx = max8_x(Sa, Sb);
        SB();
        SMF_V(av[0], A[3], W[2][0][3]); SMF_V(av[1], A[3], W[2][1][3]);
        const float nmx = mx * -sc2;
        float pa[4], pb[4];
        pa[0] = __builtin_amdgcn_exp2f(__builtin_fmaf(Sa[0], sc2, nmx)); pa[1] = __builtin_amdgcn_exp2f(__builtin_fmaf(Sa[1], sc2, nmx));
        SB();
        SMF_V(av[0], A[4], W[2][0][4]);
        pa[2] = __builtin_amdgcn_exp2f(__builtin_fmaf(Sa[2], sc2, nmx)); pa[3] = __builtin_amdgcn_exp2f(__builtin_fmaf(Sa[3], sc2, nmx));
        SB();
        MF0(Sm, k0[0], qf[0]); MFA(Sm, k0[1], qf[1]);
        pb[0] = __builtin_amdgcn_exp2f(__builtin_fmaf(Sb[0], sc2, nmx)); pb[1] = __builtin_amdgcn_exp2f(__builtin_fmaf(Sb[1], sc2, nmx));
        SB();
        MFA(Sm, k0[2], qf[2]); MFA(Sm, k0[3], qf[3]);
        pb[2] = __builtin_amdgcn_exp2f(__builtin_fmaf(Sb[2], sc2, nmx)); pb[3] = __builtin_amdgcn_exp2f(__builtin_fmaf(Sb[3], sc2, nmx));
        SB();
        SMF_V(av[1], A[4], W[2][1][4]);
        u32x4 px = {pk2(pa[0], pa[1]), pk2(pa[2], pa[3]), pk2(pb[0], pb[1]), pk2(pb[2], pb[3])};
        SB();
        SMF_V(av[0], A[5], W[2][0][5]); SMF_V(av[1], A[5], W[2][1][5]);
        u32x8 wo[4][2];
#define LDWO(tp, kk2) do { const u32x4 lo = lds_rd16(wob + (unsigned)(((tp) * 4 + 2 * (kk2)) * 1024)); const u32x4 hi = lds_rd16(wob + (unsigned)(((tp) * 4 + 2 * (kk2) + 1) * 1024)); \
            wo[tp][kk2] = (u32x8){lo.x, lo.y, lo.z, lo.w, hi.x, hi.y, hi.z, hi.w}; } while (0)
        LDWO(0, 0); LDWO(1, 0); LDWO(2, 0);
        SB();
        CT(4);
        f32x4 o[2];
        asm volatile("s_nop 1" : "+v"(px), "+v"(Sm));
        const float m = max2_x(Sm[0], Sm[1], mx);
        MF0(o[0], vx[0], px); MF0(o[1], vx[1], px);
        SB();
        const float nm = m * -sc2;
        const float pm_0 = __builtin_amdgcn_exp2f(__builtin_fmaf(Sm[0], sc2, nm)), pm_1 = __builtin_amdgcn_exp2f(__builtin_fmaf(Sm[1], sc2, nm));
        const float al = __builtin_amdgcn_exp2f(__builtin_fmaf(mx, sc2, nm));
        SB();
        SMF_V(av[0], A[6], wvl[0][0]); SMF_V(av[1], A[6], wvl[1][0]);
        const float lx = sum8_x(pa, pb);
        SB();
        SMF_V(av[0], A[7], wvl[0][1]); SMF_V(av[1], A[7], wvl[1][1]);
        SB();
        LDWO(3, 0); LDWO(0, 1); LDWO(1, 1); LDWO(2, 1); LDWO(3, 1);
        SB();
        const float rl = recip_l(pm_0, pm_1, al, lx);
        const float als = al * rl;
        { const unsigned pmp = pk2(pm_0 * rl, pm_1 * rl); pm0.x = pmp; pm1.y = pmp; }
        asm volatile("" : "+v"(o[0]), "+v"(o[1]));
#pragma unroll
        for (int r = 0; r < 4; ++r) { o[0][r] *= als; o[1][r] *= als; }
        f32x4 pp[4];
#pragma unroll
        for (int tp = 0; tp < 4; ++tp) pp[tp] = (f32x4){0, 0, 0, 0};
        SB();
        asm volatile("" : "+v"(av[0]), "+v"(av[1]));
        { unsigned r0, r1; pack_cols(av[0], av[1], r0, r1); vmt.x = r0; vmt.y = r1; }
        CT(5);
        asm volatile("s_nop 1" : "+v"(pm0), "+v"(pm1), "+v"(vmt), "+v"(o[0]), "+v"(o[1]));
        MFA(o[0], vmt, pm0); MFA(o[1], vmt, pm1);
        lds_wr8(L_VM + (unsigned)(w * 512) + (l16 >> 1), (u32x2){vmt.x, vmt.y});
        asm volatile("s_nop 6" : "+v"(o[0]), "+v"(o[1]));
        lds_wr16(owo, pack_o(o[0], o[1]));
        CT(6);
        wg_barrier();
        CT(7);
        const u32x4 oa0 = lds_rd16(L_O + l16);
        const u32x4 oa1 = lds_rd16(L_O + 1024 + l16);
        asm volatile("s_nop 1" : "+v"(pp[0]), "+v"(pp[1]), "+v"(pp[2]), "+v"(pp[3]));
        SMF_V(pp[0], oa0, wo[0][0]); SMF_V(pp[1], oa0, wo[1][0]); SMF_V(pp[2], oa0, wo[2][0]); SMF_V(pp[3], oa0, wo[3][0]);
        SMF_V(pp[0], oa1, wo[0][1]); SMF_V(pp[1], oa1, wo[1][1]); SMF_V(pp[2], oa1, wo[2][1]); SMF_V(pp[3], oa1, wo[3][1]);
        asm volatile("s_nop 5" : "+v"(pp[0]), "+v"(pp[1]));
#pragma unroll
        for (int u = 0; u < 2; ++u) {
            if (u == 1) asm volatile("" : "+v"(pp[2]), "+v"(pp[3]));
            const u32x4 v = part_pack(pp[2 * u], pp[2 * u + 1], 0xFFFFFFFEu);
            __builtin_amdgcn_raw_buffer_store_b128(v, ws_r, pvo + (unsigned)(u * 4 * 8192), (unsigned)(WS_PART + (size_t)(s % RING) * 64 * 8192), 16);
        }
        SB();
        CT(8);
        STAMP(1);
        int ln = lane; asm volatile("" : "+v"(ln));
        if (w == 3) {
            if (nh == 0) {
#pragma unroll
                for (int q2 = 0; q2 < 2; ++q2) {
                    const int ch = q2 * 64 + ln;
                    const u32x4 v = lds_rd16(kxb + 8192 + (unsigned)((((ch >> 5) * 64) + ((ch >> 3) & 3) * 16 + (ch & 7)) * 16));
                    __builtin_amdgcn_raw_buffer_store_b128(v, ws_r, (unsigned)(WS_KM + (size_t)((s % KVR) * 32 + h) * 2048) + ch * 16, 0, 16);
                }
            } else {
#pragma unroll
                for (int q2 = 0; q2 < 2; ++q2) {
                    const u32x4 v = lds_rd16(L_VM + (unsigned)((q2 * 64 + ln) * 16));
                    __builtin_amdgcn_raw_buffer_store_b128(v, ws_r, (unsigned)(WS_VM + (size_t)((s % KVR) * 32 + h) * 2048) + (q2 * 64 + ln) * 16, 0, 16);
                }
            }
            asm volatile("s_waitcnt vmcnt(0)" ::: "memory");
            if (ln == 0) __hip_atomic_store(flags + (nh == 0 ? FL_KM : FL_VM) + (s * 32 + h) * FS, 1u, RLX_AGENT);
        }
        if (s == 63) break;
        if (w == 1 || w == 2) {
            if (__builtin_amdgcn_readfirstlane(kvflag) == 0xFFFFFFFFu) wait_flag(flags + FL_KV + ((s + 1) * 32 + h) * FS, tmo);
            const unsigned src = (unsigned)((w == 1 ? WS_KX : WS_VX) + (size_t)(((s + 1) % KVR) * 32 + h) * 8192);
            const unsigned dst = (w == 1 ? L_KX + (unsigned)((s + 1) & 1) * 12288 : L_VX + (unsigned)((s + 1) & 1) * 8192);
#pragma unroll
            for (int k = 0; k < 8; ++k) dma16(ws_r, src + k * 1024 + ln * 16, dst + k * 1024);
        }
        if (w == 0) {
            for (int d = 0; d < a.dA; ++d) __builtin_amdgcn_s_sleep(1);
            const unsigned pbase = (unsigned)(WS_PART + (size_t)(s % RING) * 64 * 8192) + (unsigned)b * 8192;
            const unsigned loff = (unsigned)((((2 * (ln >> 5) + ((ln >> 3) & 1)) * 2 + ((ln >> 4) & 1)) * 8 + (ln & 7)) * 16);
            u32x4 p[8];
            STAMP(5);
            for (unsigned spins = 0;; ++spins) {
                asm volatile("" ::: "memory");
#pragma unroll
                for (int k = 0; k < 8; ++k) p[k] = __builtin_amdgcn_raw_buffer_load_b128(ws_r, pbase + k * 1024 + loff, 0, 16);
                unsigned mx = 0;
#pragma unroll
                for (int k = 0; k < 8; ++k) mx = max(mx, max4(p[k]));
                if (__all(mx != ~0u)) break;
                if (spins > SPIN_CAP) { __hip_atomic_store(tmo, 2u, RLX_AGENT); break; }
            }
            STAMP(2);
            float f[4] = {0, 0, 0, 0};
#pragma unroll
            for (int k = 0; k < 8; ++k)
#pragma unroll
                for (int e = 0; e < 4; ++e) f[e] += __uint_as_float((unsigned)p[k][e]);
#pragma unroll
            for (int e = 0; e < 4; ++e) { f[e] += xor8f(f[e]); f[e] += xor32f(f[e]); }
            unsigned d0 = pk2(f[0], f[1]), d1 = pk2(f[2], f[3]);
            unsigned dv = (ln & 8) ? d1 : d0;
            dv = dv == ~0u ? 0x7fc07fc0u : dv;
            if (ln < 32) __builtin_amdgcn_raw_buffer_store_b32(dv, ws_r, (unsigned)(WS_MEMS + (size_t)(s + 1) * 8192) + (unsigned)(b * 128 + ln * 4), 0, 16);
            if (ln == 0) lds_wr4(L_FLAG, (unsigned)(s + 1));
            STAMP(3);
            if (s == a.stop) return;
            const u32x4 pz = {~0u, ~0u, ~0u, ~0u};
#pragma unroll
            for (int k = 0; k < 8; ++k) __builtin_amdgcn_raw_buffer_store_b128(pz, ws_r, pbase + k * 1024 + ln * 16, 0, 16);
        } else {
            if (s == a.stop) return;
            for (unsigned spins = 0; lds_rd4(L_FLAG) != (unsigned)(s + 1); ++spins) { asm volatile("" ::: "memory"); __builtin_amdgcn_s_sleep(1); if (spins > SPIN_CAP) break; }
        }
        ZACC();
        {
            for (int d = 0; d < a.dB; ++d) __builtin_amdgcn_s_sleep(1);
            const unsigned mo = (unsigned)(WS_MEMS + (size_t)(s + 1) * 8192) + (unsigned)(w * 2048 + ln * 16);
            u32x4 m0, m1;
            for (unsigned spins = 0;; ++spins) {
                asm volatile("" ::: "memory");
                m0 = __builtin_amdgcn_raw_buffer_load_b128(ws_r, mo, 0, 16);
                m1 = __builtin_amdgcn_raw_buffer_load_b128(ws_r, mo + 1024, 0, 16);
                if (__all(max(max4(m0), max4(m1)) != ~0u)) break;
                if (spins > SPIN_CAP) { __hip_atomic_store(tmo, 3u, RLX_AGENT); break; }
            }
            STAMP(4);
            lds_wr16(L_MEM + (unsigned)(w * 2048 + ln * 16), m0);
            lds_wr16(L_MEM + (unsigned)(w * 2048 + 1024 + ln * 16), m1);
            asm volatile("s_waitcnt vmcnt(0)" ::: "memory");
            wg_barrier();
        }
    }
}

template <int NATURAL, int PIN, int KMODE = 0>
__device__ __forceinline__ void load_panel(const bf16* Wp, const int ld, u32x4 (&F)[2][16], const int w, const int lane) {
    const int g = lane >> 4, c = lane & 15;
#pragma unroll
    for (int it = 0; it < 4; ++it) {
        const int kk = 4 * w + it;
        u32x4 r[8], f[8];
#pragma unroll
        for (int j = 0; j < 8; ++j) r[j] = *(const u32x4*)(Wp + (size_t)(KMODE ? (32 * kk + 16 * (j >> 2) + 4 * g + (j & 3)) : (32 * kk + 8 * g + j)) * ld + 8 * c);
        tr8x8(r, f);
#pragma unroll
        for (int t = 0; t < 8; ++t) lds_wr16((unsigned)((t * 1026 + kk * 64 + lane) * 16), f[t]);
    }
    wg_barrier();
#pragma unroll
    for (int tau = 0; tau < 2; ++tau)
#pragma unroll
        for (int kk = 0; kk < 16; ++kk) {
            const int T = 2 * w + tau;
            const unsigned ad = NATURAL ? (unsigned)(((c & 7) * 1026 + kk * 64 + g * 16 + 2 * T + (c >> 3)) * 16) : (unsigned)((T * 1026 + kk * 64 + lane) * 16);
            F[tau][kk] = lds_rd16(ad);
            if (PIN) asm volatile("" : "+a"(F[tau][kk]));
        }
    wg_barrier();
}
#define MFD(acc, A_, B_, CA, CB) asm volatile("v_mfma_f32_16x16x32_bf16 %0, %1, %2, %0" : "+v"(acc) : CA(A_), CB(B_))

constexpr unsigned H_X = 0;
constexpr unsigned H_XSZ = 32 * 1040;
constexpr unsigned H_ST = 2 * H_XSZ;
constexpr unsigned H_KM = H_ST + 3 * 24576;
constexpr unsigned H_VM = H_KM + 4096;

__device__ __forceinline__ void head_role(const ChainArgs& a, const int u) {
    const int h = u >> 1, par = u & 1;
    const int tid = threadIdx.x, lane = tid & 63, w = __builtin_amdgcn_readfirstlane(tid >> 6);
    const int g = lane >> 4, c = lane & 15;
    const __amdgpu_buffer_rsrc_t ws_r = __builtin_amdgcn_make_buffer_rsrc(a.ws, 0, 0x7fffffff, 0x00020000);
    const __amdgpu_buffer_rsrc_t x_r = __builtin_amdgcn_make_buffer_rsrc((void*)a.x, 0, 2048 * 512 * 2, 0x00020000);
    gu32* flags = (gu32*)(a.ws + WS_FLAG);
    gu32* tmo = flags + FL_TMO;
    u32x4 FK[2][16], FV[2][16], FQ[2][16];
    load_panel<1, 1>(a.Wk + h * 128, 4096, FK, w, lane);
    load_panel<0, 0>(a.Wv + h * 128, 4096, FV, w, lane);
    load_panel<1, 1>(a.Wq + h * 128, 4096, FQ, w, lane);
    const float sc2 = 0.08838834764831845f * 1.4426950408889634f;
    auto load_x = [&](int s, int buf) {
#pragma unroll
        for (int r = 0; r < 8; ++r) {
            unsigned keep;
            const unsigned voff = (unsigned)(((s * 32 + 8 * w + r) * 512) * 2 + lane * 16), dst = H_X + (unsigned)buf * H_XSZ + (unsigned)(8 * w + r) * 1040;
            asm volatile("s_mov_b32 %0, m0\n\ts_mov_b32 m0, %3\n\ts_nop 0\n\tbuffer_load_dwordx4 %1, %2, 0 offen lds\n\ts_mov_b32 m0, %0" : "=&s"(keep) : "v"(voff), "s"(x_r), "s"(dst) : "memory");
        }
    };
    load_x(par, 0);
    for (int i = 0; i < 32 + 2; ++i) {
        if (i < 32) {
            const int s = 2 * i + par;
            asm volatile("s_waitcnt vmcnt(0)" ::: "memory");
            wg_barrier();
            if (i + 1 < 32) load_x(s + 2, (i + 1) & 1);
            const unsigned xb = H_X + (unsigned)(i & 1) * H_XSZ + (unsigned)(c * 1040 + g * 16);
            f32x4 dk[2][2], dq[2][2], dv[2][2];
#pragma unroll
            for (int t = 0; t < 2; ++t)
#pragma unroll
                for (int tt = 0; tt < 2; ++tt) { dk[t][tt] = (f32x4){0, 0, 0, 0}; dq[t][tt] = dk[t][tt]; dv[t][tt] = dk[t][tt]; }
            asm volatile("s_nop 1" : "+v"(dk[0][0]), "+v"(dk[0][1]), "+v"(dk[1][0]), "+v"(dk[1][1]), "+v"(dq[0][0]), "+v"(dq[0][1]), "+v"(dq[1][0]), "+v"(dq[1][1]));
            asm volatile("s_nop 1" : "+v"(dv[0][0]), "+v"(dv[0][1]), "+v"(dv[1][0]), "+v"(dv[1][1]));
#pragma unroll
            for (int kk = 0; kk < 16; ++kk) {
                const u32x4 x0 = lds_rd16(xb + (unsigned)(kk * 64)), x1 = lds_rd16(xb + (unsigned)(16 * 1040 + kk * 64));
#pragma unroll
                for (int t = 0; t < 2; ++t) {
                    MFD(dk[t][0], FK[t][kk], x0, "a", "v"); MFD(dk[t][1], FK[t][kk], x1, "a", "v");
                    MFD(dv[t][0], x0, FV[t][kk], "v", "v"); MFD(dv[t][1], x1, FV[t][kk], "v", "v");
                    MFD(dq[t][0], FQ[t][kk], x0, "a", "v"); MFD(dq[t][1], FQ[t][kk], x1, "a", "v");
                }
            }
            asm volatile("s_nop 7" : "+v"(dk[0][0]), "+v"(dk[0][1]), "+v"(dk[1][0]), "+v"(dk[1][1]), "+v"(dq[0][0]), "+v"(dq[0][1]), "+v"(dq[1][0]), "+v"(dq[1][1]));
            asm volatile("" : "+v"(dv[0][0]), "+v"(dv[0][1]), "+v"(dv[1][0]), "+v"(dv[1][1]));
            const unsigned st = H_ST + (unsigned)(i % 3) * 24576;
            const unsigned gk = (unsigned)(WS_KX + (size_t)((s % KVR) * 32 + h) * 8192), gv = (unsigned)(WS_VX + (size_t)((s % KVR) * 32 + h) * 8192);
#pragma unroll
            for (int tt = 0; tt < 2; ++tt) {
                const u32x4 kf = {pk2(dk[0][tt][0], dk[0][tt][1]), pk2(dk[0][tt][2], dk[0][tt][3]), pk2(dk[1][tt][0], dk[1][tt][1]), pk2(dk[1][tt][2], dk[1][tt][3])};
                const u32x4 qf = {pk2(dq[0][tt][0], dq[0][tt][1]), pk2(dq[0][tt][2], dq[0][tt][3]), pk2(dq[1][tt][0], dq[1][tt][1]), pk2(dq[1][tt][2], dq[1][tt][3])};
                const unsigned o = (unsigned)((tt * 4 + w) * 1024 + lane * 16);
                __builtin_amdgcn_raw_buffer_store_b128(kf, ws_r, gk + o, 0, 16);
                lds_wr16(st + o, qf); lds_wr16(st + 8192 + o, kf);
            }
#pragma unroll
            for (int t = 0; t < 2; ++t) {
                const u32x4 vf = {pk2(dv[t][0][0], dv[t][0][1]), pk2(dv[t][0][2], dv[t][0][3]), pk2(dv[t][1][0], dv[t][1][1]), pk2(dv[t][1][2], dv[t][1][3])};
                const unsigned o = (unsigned)((2 * w + t) * 1024 + lane * 16);
                __builtin_amdgcn_raw_buffer_store_b128(vf, ws_r, gv + o, 0, 16);
                lds_wr16(st + 16384 + o, vf);
            }
            asm volatile("s_waitcnt vmcnt(0)" ::: "memory");
            wg_barrier();
            if (tid == 0) __hip_atomic_store(flags + FL_KV + (s * 32 + h) * FS, 1u, RLX_AGENT);
        }
        if (i >= 2 && a.pad == 0) {
            const int s = 2 * (i - 2) + par;
            const unsigned st = H_ST + (unsigned)((i - 2) % 3) * 24576;
            if (w == 0) {
                wait_flag(flags + FL_KM + (s * 32 + h) * FS, tmo);
#pragma unroll
                for (int q2 = 0; q2 < 2; ++q2) {
                    const int ch = q2 * 64 + lane;
                    const u32x4 v = __builtin_amdgcn_raw_buffer_load_b128(ws_r, (unsigned)(WS_KM + (size_t)((s % KVR) * 32 + h) * 2048) + ch * 16, 0, 16);
                    lds_wr16(H_KM + (unsigned)((((ch >> 5) * 64) + ((ch >> 3) & 3) * 16 + (ch & 7)) * 16), v);
                }
            } else if (w == 1) {
                wait_flag(flags + FL_VM + (s * 32 + h) * FS, tmo);
#pragma unroll
                for (int q2 = 0; q2 < 2; ++q2) {
                    const u32x4 v = __builtin_amdgcn_raw_buffer_load_b128(ws_r, (unsigned)(WS_VM + (size_t)((s % KVR) * 32 + h) * 2048) + (q2 * 64 + lane) * 16, 0, 16);
                    lds_wr16(H_VM + (unsigned)((q2 * 64 + lane) * 16), v);
                }
            }
            wg_barrier();
            const int qt = w & 1, eh = w >> 1;
            f32x4 Sm, Sa, Sb;
            const unsigned km = H_KM + (unsigned)((g * 16 + 2 * (c >> 2) + (c & 1)) * 16);
            {
                u32x4 q8[4], kmf[4], kaf[4], kbf[4];
#pragma unroll
                for (int ks = 0; ks < 4; ++ks) {
                    q8[ks] = lds_rd16(st + (unsigned)((qt * 4 + ks) * 1024 + lane * 16));
                    kmf[ks] = lds_rd16(km + ks * 1024);
                    kaf[ks] = lds_rd16(st + 8192 + (unsigned)(ks * 1024 + lane * 16));
                    kbf[ks] = lds_rd16(st + 8192 + (unsigned)((4 + ks) * 1024 + lane * 16));
                }
                MF0(Sm, kmf[0], q8[0]); MF0(Sa, kaf[0], q8[0]); MF0(Sb, kbf[0], q8[0]);
#pragma unroll
                for (int ks = 1; ks < 4; ++ks) { MFA(Sm, kmf[ks], q8[ks]); MFA(Sa, kaf[ks], q8[ks]); MFA(Sb, kbf[ks], q8[ks]); }
                asm volatile("s_nop 7" : "+v"(Sm), "+v"(Sa), "+v"(Sb));
            }
            const float NEG = -3.0e38f;
            float sa[4], sb[4];
#pragma unroll
            for (int r = 0; r < 4; ++r) {
                const bool diag = (4 * g + r) <= c;
                sa[r] = (qt == 1 || diag) ? Sa[r] : NEG;
                sb[r] = (qt == 1 && diag) ? Sb[r] : NEG;
            }
            float m = fmaxf(fmaxf(fmaxf(sa[0], sa[1]), fmaxf(sa[2], sa[3])), fmaxf(fmaxf(sb[0], sb[1]), fmaxf(sb[2], sb[3])));
            m = fmaxf(m, fmaxf(Sm[0], Sm[1]));
            m = fmaxf(m, xor16f(m)); m = fmaxf(m, xor32f(m));
            const float nm = -m * sc2;
            float pa[4], pb[4];
#pragma unroll
            for (int r = 0; r < 4; ++r) {
                pa[r] = sa[r] == NEG ? 0.f : __builtin_amdgcn_exp2f(__builtin_fmaf(sa[r], sc2, nm));
                pb[r] = sb[r] == NEG ? 0.f : __builtin_amdgcn_exp2f(__builtin_fmaf(sb[r], sc2, nm));
            }
            const float pm0 = __builtin_amdgcn_exp2f(__builtin_fmaf(Sm[0], sc2, nm)), pm1 = __builtin_amdgcn_exp2f(__builtin_fmaf(Sm[1], sc2, nm));
            float l = ((pa[0] + pa[1]) + (pa[2] + pa[3])) + ((pb[0] + pb[1]) + (pb[2] + pb[3])) + (pm0 + pm1);
            l += xor16f(l); l += xor32f(l);
            const float rl = __builtin_amdgcn_rcpf(l);
            u32x4 px = {pk2(pa[0] * rl, pa[1] * rl), pk2(pa[2] * rl, pa[3] * rl), pk2(pb[0] * rl, pb[1] * rl), pk2(pb[2] * rl, pb[3] * rl)};
            u32x4 pmv = {pk2(pm0 * rl, pm1 * rl), 0u, 0u, 0u};
            f32x4 o[4];
#pragma unroll
            for (int tq = 0; tq < 4; ++tq) {
                const int t = 4 * eh + tq;
                const u32x4 vx = lds_rd16(st + 16384 + (unsigned)(t * 1024 + lane * 16));
                const unsigned vm = lds_rd4(H_VM + (unsigned)(((t >> 1) * 64 + lane) * 8 + (t & 1) * 4));
                u32x4 va = {vm, 0u, 0u, 0u};
                asm volatile("s_nop 1" : "+v"(va), "+v"(px), "+v"(pmv));
                MF0(o[tq], vx, px);
                MFA(o[tq], va, pmv);
            }
            asm volatile("s_nop 7" : "+v"(o[0]), "+v"(o[1]), "+v"(o[2]), "+v"(o[3]));
            const unsigned go = (unsigned)(WS_OX + (size_t)((s * 2 + qt) * 128 + h * 4 + eh * 2) * 1024) + (unsigned)lane * 16;
#pragma unroll
            for (int f = 0; f < 2; ++f) {
                const u32x4 v = {pk2(o[0][2 * f], o[1][2 * f]), pk2(o[2][2 * f], o[3][2 * f]), pk2(o[0][2 * f + 1], o[1][2 * f + 1]), pk2(o[2][2 * f + 1], o[3][2 * f + 1])};
                __builtin_amdgcn_raw_buffer_store_b128(v, ws_r, go + f * 1024, 0, 16);
            }
            asm volatile("s_waitcnt vmcnt(0)" ::: "memory");
            wg_barrier();
            if (tid == 0) __hip_atomic_fetch_add(flags + FL_O + s * 32, 1u, RLX_AGENT);
        }
    }
}

constexpr int NREP = 3;
__device__ __forceinline__ void oproj_role(const ChainArgs& a, const int u) {
    const int n = u & 15, rp = u >> 4;
    const int tid = threadIdx.x, lane = tid & 63, w = __builtin_amdgcn_readfirstlane(tid >> 6);
    const int g = lane >> 4, c = lane & 15;
    const __amdgpu_buffer_rsrc_t ws_r = __builtin_amdgcn_make_buffer_rsrc(a.ws, 0, 0x7fffffff, 0x00020000);
    gu32* flags = (gu32*)(a.ws + WS_FLAG);
    gu32* tmo = flags + FL_TMO;
    u32x4 F[32][2];
    {
        const int kq = lane >> 4, gg = (lane >> 2) & 3, oct = lane & 3;
        const unsigned lb = (unsigned)w * 16384;
#pragma unroll
        for (int it = 0; it < 8; ++it) {
            const int kk = 32 * w + 4 * it + kq;
            const int hh = kk >> 2, eh = (kk >> 1) & 1, f = kk & 1;
            const bf16* Wp = a.Wo + (size_t)(hh * 128 + 32 * gg + 16 * f + 4 * eh) * 512 + 32 * n + 8 * oct;
            u32x4 r[8], fr[8];
#pragma unroll
            for (int j = 0; j < 8; ++j) r[j] = *(const u32x4*)(Wp + (size_t)(8 * (j >> 2) + (j & 3)) * 512);
            tr8x8(r, fr);
#pragma unroll
            for (int t = 0; t < 8; ++t) lds_wr16(lb + (unsigned)((((kq * 2 + (oct >> 1)) * 4 + gg) * 16 + 8 * (oct & 1) + t) * 16), fr[t]);
            asm volatile("s_waitcnt lgkmcnt(0)" ::: "memory");
#pragma unroll
            for (int q4 = 0; q4 < 4; ++q4)
#pragma unroll
                for (int T = 0; T < 2; ++T) { F[4 * it + q4][T] = lds_rd16(lb + (unsigned)(((q4 * 2 + T) * 64 + lane) * 16)); asm volatile("" : "+a"(F[4 * it + q4][T])); }
            asm volatile("s_waitcnt lgkmcnt(0)" ::: "memory");
        }
    }
    wg_barrier();
    for (int un = rp; un < 128; un += NREP) {
        const int s = un >> 1, rh = un & 1;
        if (w == 0) {
            gu32* f = flags + FL_O + s * 32;
            for (unsigned spins = 0;; ++spins) {
                const unsigned v = __builtin_amdgcn_readfirstlane(__hip_atomic_load(f, RLX_AGENT));
                if (v == 31u) break;
                if (spins > SPIN_CAP) { __hip_atomic_store(tmo, 4u, RLX_AGENT); break; }
                __builtin_amdgcn_s_sleep(PSLEEP * 2);
            }
        }
        wg_barrier();
        const unsigned ab = (unsigned)(WS_OX + (size_t)((s * 2 + rh) * 128 + 32 * w) * 1024) + (unsigned)lane * 16;
        f32x4 d0 = {0, 0, 0, 0}, d1 = d0;
        asm volatile("s_nop 1" : "+v"(d0), "+v"(d1));
#pragma unroll
        for (int hb = 0; hb < 2; ++hb) {
            u32x4 A[16];
#pragma unroll
            for (int i = 0; i < 16; ++i) A[i] = __builtin_amdgcn_raw_buffer_load_b128(ws_r, ab + (unsigned)((16 * hb + i) * 1024), 0, 16);
#pragma unroll
            for (int i = 0; i < 16; ++i) { MFD(d0, A[i], F[16 * hb + i][0], "v", "a"); MFD(d1, A[i], F[16 * hb + i][1], "v", "a"); }
        }
        asm volatile("s_nop 7" : "+v"(d0), "+v"(d1));
        lds_wr16(65536u + (unsigned)(((w * 2 + 0) * 64 + lane) * 16), __builtin_bit_cast(u32x4, d0));
        lds_wr16(65536u + (unsigned)(((w * 2 + 1) * 64 + lane) * 16), __builtin_bit_cast(u32x4, d1));
        wg_barrier();
        {
            const int tok = tid >> 4, cp = tid & 15, T = cp >> 3, c0 = (2 * cp) & 15;
            float v0 = 0, v1 = 0;
#pragma unroll
            for (int ww = 0; ww < 4; ++ww) {
                const unsigned ad = 65536u + (unsigned)((((ww * 2 + T) * 64) + (tok >> 2) * 16 + c0) * 16 + (tok & 3) * 4);
                v0 += __uint_as_float(lds_rd4(ad)); v1 += __uint_as_float(lds_rd4(ad + 16));
            }
            *(unsigned*)(a.out + (size_t)(s * 32 + rh * 16 + tok) * 1024 + 512 + 32 * n + 2 * cp) = pk2(v0, v1);
        }
        wg_barrier();
    }
}

__device__ __forceinline__ void wait_count(gu32* f, unsigned want, gu32* tmo) {
    for (unsigned spins = 0;; ++spins) {
        const unsigned v = __builtin_amdgcn_readfirstlane(__hip_atomic_load(f, RLX_AGENT));
        if (v == want) return;
        if (spins > SPIN_CAP) { __hip_atomic_store(tmo, 5u, RLX_AGENT); return; }
        __builtin_amdgcn_s_sleep(8);
    }
}
template <int NV>
__device__ __forceinline__ void a1_unit(const ChainArgs& a, const int j, const int rg) {
    const int tid = threadIdx.x, lane = tid & 63, w = __builtin_amdgcn_readfirstlane(tid >> 6);
    const int g = lane >> 4, c = lane & 15;
    const __amdgpu_buffer_rsrc_t ws_r = __builtin_amdgcn_make_buffer_rsrc(a.ws, 0, 0x7fffffff, 0x00020000);
    const __amdgpu_buffer_rsrc_t x_r = __builtin_amdgcn_make_buffer_rsrc((void*)a.ax, 0, 2048 * 512 * 2, 0x00020000);
    u32x4 F0[2][16], F1[2][16], F2[2][16];
    load_panel<1, 1>(a.awqkv + 384 * j, 1536, F0, w, lane);
    load_panel<1, 1>(a.awqkv + 384 * j + 128, 1536, F1, w, lane);
    load_panel<1, 0>(a.awqkv + 384 * j + 256, 1536, F2, w, lane);
    auto load_x = [&](int t32, int buf) {
#pragma unroll
        for (int r = 0; r < 8; ++r) {
            unsigned keep;
            const unsigned voff = (unsigned)(((t32 * 32 + 8 * w + r) * 512) * 2 + lane * 16), dst = H_X + (unsigned)buf * H_XSZ + (unsigned)(8 * w + r) * 1040;
            asm volatile("s_mov_b32 %0, m0\n\ts_mov_b32 m0, %3\n\ts_nop 0\n\tbuffer_load_dwordx4 %1, %2, 0 offen lds\n\ts_mov_b32 m0, %0" : "=&s"(keep) : "v"(voff), "s"(x_r), "s"(dst) : "memory");
        }
    };
    load_x(rg * 4, 0);
    for (int i = 0; i < 4; ++i) {
        asm volatile("s_waitcnt vmcnt(0)" ::: "memory");
        wg_barrier();
        if (i + 1 < 4) load_x(rg * 4 + i + 1, (i + 1) & 1);
        const unsigned xb = H_X + (unsigned)(i & 1) * H_XSZ + (unsigned)(c * 1040 + g * 16);
        f32x4 d[3][2][2];
#pragma unroll
        for (int p = 0; p < 3; ++p)
#pragma unroll
            for (int t = 0; t < 2; ++t) { d[p][t][0] = (f32x4){0, 0, 0, 0}; d[p][t][1] = d[p][t][0]; }
        asm volatile("s_nop 1" : "+v"(d[0][0][0]), "+v"(d[0][0][1]), "+v"(d[0][1][0]), "+v"(d[0][1][1]), "+v"(d[1][0][0]), "+v"(d[1][0][1]), "+v"(d[1][1][0]), "+v"(d[1][1][1]));
        asm volatile("s_nop 1" : "+v"(d[2][0][0]), "+v"(d[2][0][1]), "+v"(d[2][1][0]), "+v"(d[2][1][1]));
#pragma unroll
        for (int kk = 0; kk < 16; ++kk) {
            const u32x4 x0 = lds_rd16(xb + (unsigned)(kk * 64)), x1 = lds_rd16(xb + (unsigned)(16 * 1040 + kk * 64));
#pragma unroll
            for (int t = 0; t < 2; ++t) {
                if (NV >= 3) { MFD(d[0][t][0], x0, F0[t][kk], "v", "a"); MFD(d[0][t][1], x1, F0[t][kk], "v", "a"); MFD(d[1][t][0], x0, F1[t][kk], "v", "a"); MFD(d[1][t][1], x1, F1[t][kk], "v", "a"); }
                else { MFD(d[0][t][0], F0[t][kk], x0, "a", "v"); MFD(d[0][t][1], F0[t][kk], x1, "a", "v"); MFD(d[1][t][0], F1[t][kk], x0, "a", "v"); MFD(d[1][t][1], F1[t][kk], x1, "a", "v"); }
                if (NV >= 1) { MFD(d[2][t][0], x0, F2[t][kk], "v", "v"); MFD(d[2][t][1], x1, F2[t][kk], "v", "v"); }
                else { MFD(d[2][t][0], F2[t][kk], x0, "v", "v"); MFD(d[2][t][1], F2[t][kk], x1, "v", "v"); }
            }
        }
        asm volatile("s_nop 7" : "+v"(d[0][0][0]), "+v"(d[0][0][1]), "+v"(d[0][1][0]), "+v"(d[0][1][1]), "+v"(d[1][0][0]), "+v"(d[1][0][1]), "+v"(d[1][1][0]), "+v"(d[1][1][1]));
        asm volatile("" : "+v"(d[2][0][0]), "+v"(d[2][0][1]), "+v"(d[2][1][0]), "+v"(d[2][1][1]));
#pragma unroll
        for (int p = 0; p < 3; ++p)
#pragma unroll
            for (int t = 0; t < 2; ++t)
#pragma unroll
                for (int tt = 0; tt < 2; ++tt) {
                    const int hd = (384 * j + 128 * p) / 16 + 2 * w + t;
                    const int tile = (rg * 4 + i) * 2 + tt;
                    const u32x2 v = {pk2(d[p][t][tt][0], d[p][t][tt][1]), pk2(d[p][t][tt][2], d[p][t][tt][3])};
                    __builtin_amdgcn_raw_buffer_store_b64(v, ws_r, (unsigned)(WS_AQ + (size_t)(hd * 128 + tile) * 512) + (unsigned)lane * 8, 0, 16);
                }
    }
    asm volatile("s_waitcnt vmcnt(0)" ::: "memory");
    wg_barrier();
    if (tid == 0) __hip_atomic_fetch_add((gu32*)(a.ws + WS_FLAG) + FL_A1 + rg * 32, 1u, RLX_AGENT);
}

__device__ __forceinline__ void a2_unit(const ChainArgs& a, const int un) {
    const int rg64 = un >> 2, ho = un & 3;
    const int tid = threadIdx.x, lane = tid & 63, w = __builtin_amdgcn_readfirstlane(tid >> 6);
    const int g = lane >> 4, c = lane & 15;
    const __amdgpu_buffer_rsrc_t ws_r = __builtin_amdgcn_make_buffer_rsrc(a.ws, 0, 0x7fffffff, 0x00020000);
    gu32* flags = (gu32*)(a.ws + WS_FLAG);
    if (w == 0) {
        const int lo = (64 * rg64 - 256) < 0 ? 0 : (64 * rg64 - 256) / 128, hi = (64 * rg64 + 63) / 128;
        for (int r = lo; r <= hi; ++r) wait_count(flags + FL_A1 + r * 32, 3u, flags + FL_TMO);
    }
    wg_barrier();
    const float sc2 = 0.25f * 1.4426950408889634f;
    for (int it = 0; it < 8; ++it) {
        const int hd = 8 * ho + 2 * w + (it >> 2), qt = 4 * rg64 + (it & 3);
        const unsigned qb = (unsigned)(WS_AQ + (size_t)(hd * 128) * 512) + (unsigned)lane * 8;
        const unsigned kb = qb + 32u * 128u * 512u, vb = kb + 32u * 128u * 512u;
        const u32x2 qf = __builtin_amdgcn_raw_buffer_load_b64(ws_r, qb + (unsigned)qt * 512, 0, 16);
        u32x2 kf[17], vf[17];
#pragma unroll
        for (int i = 0; i < 17; ++i) {
            const int kt = qt - 16 + i, ktc = kt < 0 ? 0 : kt;
            kf[i] = __builtin_amdgcn_raw_buffer_load_b64(ws_r, kb + (unsigned)ktc * 512, 0, 16);
            vf[i] = __builtin_amdgcn_raw_buffer_load_b64(ws_r, vb + (unsigned)ktc * 512, 0, 16);
        }
        f32x4 S[17];
#pragma unroll
        for (int i = 0; i < 17; ++i) {
            f32x4 z = {0, 0, 0, 0};
            S[i] = __builtin_amdgcn_mfma_f32_16x16x16bf16_1k(__builtin_bit_cast(bf16x4, kf[i]), __builtin_bit_cast(bf16x4, qf), z, 0, 0, 0);
        }
        const float NEG = -3.0e38f;
        float m = NEG;
#pragma unroll
        for (int i = 0; i < 17; ++i) {
            const int kt = qt - 16 + i;
#pragma unroll
            for (int r = 0; r < 4; ++r) {
                bool ok = kt >= 0;
                if (i == 0) ok = ok && (4 * g + r) > c;
                if (i == 16) ok = (4 * g + r) <= c;
                S[i][r] = ok ? S[i][r] : NEG;
                m = fmaxf(m, S[i][r]);
            }
        }
        m = fmaxf(m, xor16f(m)); m = fmaxf(m, xor32f(m));
        const float nm = -m * sc2;
        float l = 0;
        u32x2 P[18];
#pragma unroll
        for (int i = 0; i < 17; ++i) {
            float p[4];
#pragma unroll
            for (int r = 0; r < 4; ++r) { p[r] = S[i][r] == NEG ? 0.f : __builtin_amdgcn_exp2f(__builtin_fmaf(S[i][r], sc2, nm)); l += p[r]; }
            P[i] = (u32x2){pk2(p[0], p[1]), pk2(p[2], p[3])};
        }
        P[17] = (u32x2){0u, 0u};
        l += xor16f(l); l += xor32f(l);
        f32x4 o = {0, 0, 0, 0};
#pragma unroll
        for (int i = 0; i < 9; ++i) {
            const u32x2 v1 = (2 * i + 1 < 17) ? vf[2 * i + 1 < 17 ? 2 * i + 1 : 0] : (u32x2){0u, 0u};
            const u32x4 va = {vf[2 * i].x, vf[2 * i].y, v1.x, v1.y};
            const u32x4 pbv = {P[2 * i].x, P[2 * i].y, P[2 * i + 1].x, P[2 * i + 1].y};
            o = __builtin_amdgcn_mfma_f32_16x16x32_bf16(__builtin_bit_cast(bf16x8, va), __builtin_bit_cast(bf16x8, pbv), o, 0, 0, 0);
        }
        const float rl = __builtin_amdgcn_rcpf(l);
        const u32x2 ov = {pk2(o[0] * rl, o[1] * rl), pk2(o[2] * rl, o[3] * rl)};
        __builtin_amdgcn_raw_buffer_store_b64(ov, ws_r, (unsigned)(WS_AO + (size_t)(qt * 16 + (hd >> 1)) * 1024) + (unsigned)(lane * 16 + (hd & 1) * 8), 0, 16);
    }
    asm volatile("s_waitcnt vmcnt(0)" ::: "memory");
    wg_barrier();
    if (tid == 0) __hip_atomic_fetch_add(flags + FL_A2 + rg64 * 32, 1u, RLX_AGENT);
}

__device__ __forceinline__ void a3_unit(const ChainArgs& a, const int un) {
    const int js = un & 1, rgp = un >> 1;
    const int tid = threadIdx.x, lane = tid & 63, w = __builtin_amdgcn_readfirstlane(tid >> 6);
    const int g = lane >> 4, c = lane & 15;
    const __amdgpu_buffer_rsrc_t ws_r = __builtin_amdgcn_make_buffer_rsrc(a.ws, 0, 0x7fffffff, 0x00020000);
    gu32* flags = (gu32*)(a.ws + WS_FLAG);
    u32x4 F0[2][16], F1[2][16];
    load_panel<1, 1, 1>(a.awo + 256 * js, 512, F0, w, lane);
    load_panel<1, 1, 1>(a.awo + 256 * js + 128, 512, F1, w, lane);
    if (w == 0) { wait_count(flags + FL_A2 + (2 * rgp) * 32, 3u, flags + FL_TMO); wait_count(flags + FL_A2 + (2 * rgp + 1) * 32, 3u, flags + FL_TMO); }
    wg_barrier();
    for (int i = 0; i < 8; ++i) {
        const int tile = rgp * 8 + i;
        u32x4 A[16];
#pragma unroll
        for (int kk = 0; kk < 16; ++kk) A[kk] = __builtin_amdgcn_raw_buffer_load_b128(ws_r, (unsigned)(WS_AO + (size_t)(tile * 16 + kk) * 1024) + (unsigned)lane * 16, 0, 16);
        f32x4 d[2][2];
        d[0][0] = (f32x4){0, 0, 0, 0}; d[0][1] = d[0][0]; d[1][0] = d[0][0]; d[1][1] = d[0][0];
        asm volatile("s_nop 1" : "+v"(d[0][0]), "+v"(d[0][1]), "+v"(d[1][0]), "+v"(d[1][1]));
#pragma unroll
        for (int kk = 0; kk < 16; ++kk)
#pragma unroll
            for (int t = 0; t < 2; ++t) { MFD(d[0][t], F0[t][kk], A[kk], "a", "v"); MFD(d[1][t], F1[t][kk], A[kk], "a", "v"); }
        asm volatile("s_nop 7" : "+v"(d[0][0]), "+v"(d[0][1]), "+v"(d[1][0]), "+v"(d[1][1]));
#pragma unroll
        for (int p = 0; p < 2; ++p)
#pragma unroll
            for (int t = 0; t < 2; ++t) {
                const u32x2 v = {pk2(d[p][t][0], d[p][t][1]), pk2(d[p][t][2], d[p][t][3])};
                *(u32x2*)(a.out + (size_t)(tile * 16 + c) * 1024 + 256 * js + 128 * p + 16 * (2 * w + t) + 4 * g) = v;
            }
    }
}

__device__ __forceinline__ void a_role(const ChainArgs& a, const int ua) {
    if (ua < 64) {
        const int j = ua & 3, rg = ua >> 2;
        if (j < 2) a1_unit<0>(a, j, rg); else if (j == 2) a1_unit<1>(a, j, rg); else a1_unit<3>(a, j, rg);
    }
    wg_barrier();
    for (int un = ua; un < 128; un += 80) a2_unit(a, un);
    wg_barrier();
    if (ua >= 48) a3_unit(a, ua - 48);
}

__global__ void __launch_bounds__(256, 1) fused_kernel(ChainArgs a) {
    extern __shared__ unsigned char smem[];
    const int b = blockIdx.x;
    const int x = b & 7, slot = b >> 3;
    if (slot < 8) { if (a.pad != 1) chain_role(a, (x + 8 * (slot >> 1)) * 2 + (slot & 1)); }
    else if (a.pad == 2) return;
    else if (slot < 16) head_role(a, (x + 8 * ((slot - 8) >> 1)) * 2 + (slot & 1));
    else if (x < NREP) { if (a.pad == 0) oproj_role(a, x * 16 + (slot - 16)); }
    else a_role(a, (x - NREP) * 16 + (slot - 16));
}

#define STOP 1000
#define DA 8
#define DB 8
extern "C" void kernel_launch(void* const* d_in, const int* in_sizes, int n_in, void* d_out, int out_size, void* d_ws, size_t ws_size, hipStream_t stream) {
    const bf16* Ax = (const bf16*)d_in[0]; const bf16* Awqkv = (const bf16*)d_in[1]; const bf16* Awo = (const bf16*)d_in[2];
    const bf16* Bx = (const bf16*)d_in[3]; const bf16* mem0 = (const bf16*)d_in[4];
    const bf16* Wq = (const bf16*)d_in[5]; const bf16* Wk = (const bf16*)d_in[6]; const bf16* Wv = (const bf16*)d_in[7]; const bf16* Wo = (const bf16*)d_in[8];
    bf16* out = (bf16*)d_out;
    unsigned char* wsb = (unsigned char*)d_ws;
    bf16* ws = (bf16*)(wsb + WS_END1);
    bf16* qkv = ws; ws += (size_t)2048 * 1536;
    bf16* oa = ws; ws += (size_t)2048 * 512;
    (void)hipMemsetAsync(wsb, 0xFF, WS_INIT_BYTES, stream);
    (void)hipFuncSetAttribute((const void*)fused_kernel, hipFuncAttributeMaxDynamicSharedMemorySize, LDS_BYTES);
    ChainArgs ca{Bx, out, Ax, Awqkv, Awo, mem0, Wq, Wk, Wv, Wo, wsb, DA, DB, STOP, 0};
    fused_kernel<<<256, 256, LDS_BYTES, stream>>>(ca);
}
```

```cpp
#include <hip/hip_runtime.h>
#include <hip/hip_bf16.h>
#include <stdint.h>

typedef __hip_bfloat16 bf16;
typedef __bf16 bf16x8 __attribute__((ext_vector_type(8)));
typedef __bf16 bf16x4 __attribute__((ext_vector_type(4)));
typedef __bf16 bf16x2 __attribute__((ext_vector_type(2)));
typedef __bf16 bf16x16 __attribute__((ext_vector_type(16)));
typedef float f32x4 __attribute__((ext_vector_type(4)));
typedef float f32x2 __attribute__((ext_vector_type(2)));
typedef unsigned u32x4 __attribute__((ext_vector_type(4)));
typedef unsigned u32x2 __attribute__((ext_vector_type(2)));
typedef unsigned u32x8 __attribute__((ext_vector_type(8)));
#define LAS __attribute__((address_space(3)))
#define GAS __attribute__((address_space(1)))
typedef GAS unsigned gu32;
#define RLX_AGENT __ATOMIC_RELAXED, __HIP_MEMORY_SCOPE_AGENT

__device__ __forceinline__ float b2f(bf16 x) { return __bfloat162float(x); }
__device__ __forceinline__ bf16 f2b(float x) { return __float2bfloat16(x); }

#define FSTRIDE 32
#define PSLEEP 2
#define KVRING 64
constexpr int RING = 4;
constexpr int NVREG = 6;
constexpr size_t WS_MEMS = 0;
constexpr size_t WS_PART = WS_MEMS + 64 * 8192;
constexpr size_t WS_FLAG = WS_PART + (size_t)RING * 64 * 8192;
constexpr size_t FLAG_BYTES = 3 * 2048 * FSTRIDE * 4 + 32768;
constexpr size_t WS_INIT_BYTES = WS_FLAG + FLAG_BYTES;
constexpr size_t WS_KX = WS_INIT_BYTES;
constexpr int KVR = KVRING;
constexpr size_t WS_VX = WS_KX + (size_t)KVR * 32 * 8192;
constexpr size_t WS_KM = WS_VX + (size_t)KVR * 32 * 8192;
constexpr size_t WS_VM = WS_KM + (size_t)KVR * 32 * 2048;
constexpr size_t WS_OX = WS_VM + (size_t)KVR * 32 * 2048;
constexpr size_t WS_AQ = WS_OX + (size_t)64 * 2 * 128 * 1024;
constexpr size_t WS_AO = WS_AQ + (size_t)3 * 32 * 128 * 512;
constexpr size_t WS_END1 = WS_AO + (size_t)128 * 16 * 1024;
constexpr int FS = FSTRIDE;
constexpr int FL_KV = 0;
constexpr int FL_KM = FL_KV + 2048 * FS;
constexpr int FL_VM = FL_KM + 2048 * FS;
constexpr int FL_O = FL_VM + 2048 * FS;
constexpr int FL_A1 = FL_O + 64 * 32;
constexpr int FL_A2 = FL_A1 + 16 * 32;
constexpr int FL_TMO = FL_A2 + 32 * 32;

constexpr unsigned SPIN_CAP = 1u << 20;

constexpr unsigned L_MEM = 0;
constexpr unsigned L_KX = 8192;
constexpr unsigned L_VX = L_KX + 2 * 12288;
constexpr unsigned L_Q = L_VX + 2 * 8192;
constexpr unsigned L_O = L_Q + 4096;
constexpr unsigned L_VM = L_O + 2048;
constexpr unsigned L_FLAG = L_VM + 2048;
constexpr unsigned L_DUMMY = L_FLAG + 64;
constexpr unsigned L_WV = 65536;
constexpr unsigned L_WO = 98304;
constexpr unsigned L_STG = 0;
constexpr unsigned LDS_BYTES = 160 * 1024;

__device__ __forceinline__ u32x4 lds_rd16(unsigned a) { return *(LAS u32x4*)(size_t)a; }
__device__ __forceinline__ void lds_wr16(unsigned a, u32x4 v) { *(LAS u32x4*)(size_t)a = v; }
__device__ __forceinline__ void lds_wr4(unsigned a, unsigned v) { *(LAS unsigned*)(size_t)a = v; }
__device__ __forceinline__ void lds_wr8(unsigned a, u32x2 v) { *(LAS u32x2*)(size_t)a = v; }
__device__ __forceinline__ unsigned lds_rd4(unsigned a) { return *(LAS unsigned*)(size_t)a; }
__device__ __forceinline__ u32x2 lds_rd8(unsigned a) { return *(LAS u32x2*)(size_t)a; }
__device__ __forceinline__ void wg_barrier() { asm volatile("s_waitcnt lgkmcnt(0)" ::: "memory"); __builtin_amdgcn_s_barrier(); asm volatile("" ::: "memory"); }
__device__ __forceinline__ unsigned pk2(float a, float b) { bf16x2 v = {(__bf16)a, (__bf16)b}; return __builtin_bit_cast(unsigned, v); }
__device__ __forceinline__ unsigned max4(u32x4 a) { return max(max(a.x, a.y), max(a.z, a.w)); }
__device__ __forceinline__ void dma16(const __amdgpu_buffer_rsrc_t r, unsigned voff, unsigned lds_dst) {
    unsigned keep;
    asm volatile("s_mov_b32 %0, m0\n\ts_mov_b32 m0, %3\n\ts_nop 0\n\tbuffer_load_dwordx4 %1, %2, 0 offen sc1 lds\n\ts_mov_b32 m0, %0" : "=&s"(keep) : "v"(voff), "s"(r), "s"(lds_dst) : "memory");
}
__device__ __forceinline__ float xor8f(float v) { return __builtin_bit_cast(float, __builtin_amdgcn_update_dpp(0, __builtin_bit_cast(int, v), 0x128  , 0xf, 0xf, false)); }
__device__ __forceinline__ float xor16f(float v) {
    auto r = __builtin_amdgcn_permlane16_swap(__builtin_bit_cast(unsigned, v), __builtin_bit_cast(unsigned, v), false, false);
    return __builtin_bit_cast(float, (threadIdx.x & 16) ? r[0] : r[1]);
}
__device__ __forceinline__ float xor32f(float v) {
    auto r = __builtin_amdgcn_permlane32_swap(__builtin_bit_cast(unsigned, v), __builtin_bit_cast(unsigned, v), false, false);
    return __builtin_bit_cast(float, (threadIdx.x & 32) ? r[0] : r[1]);
}

__device__ __forceinline__ void tr8x8(const u32x4 (&r)[8], u32x4 (&f)[8]) {
#pragma unroll
    for (int t = 0; t < 8; ++t) {
        const unsigned sel = (t & 1) ? 0x07060302u : 0x05040100u;
#pragma unroll
        for (int d = 0; d < 4; ++d) f[t][d] = __builtin_amdgcn_perm(r[2 * d + 1][t >> 1], r[2 * d][t >> 1], sel);
    }
}

#define SMF_A(acc, A_, W_) asm volatile("v_smfmac_f32_16x16x64_bf16 %0, %1, %2, %3" : "+v"(acc) : "v"(A_), "a"(W_), "v"(idx))
#define SMF_V(acc, A_, W_) asm volatile("v_smfmac_f32_16x16x64_bf16 %0, %1, %2, %3" : "+v"(acc) : "v"(A_), "v"(W_), "v"(idx))
#define SMF_VN(acc, A_, W_) asm volatile("s_nop 1\n\tv_smfmac_f32_16x16x64_bf16 %0, %1, %2, %3" : "+v"(acc) : "v"(A_), "v"(W_), "v"(idx))
__device__ __forceinline__ float vadd(float a, float b) { float r; asm("v_add_f32 %0, %1, %2" : "=v"(r) : "v"(a), "v"(b)); return r; }
__device__ __forceinline__ float vmax3(float a, float b, float c) { float r; asm("v_max3_f32 %0, %1, %2, %3" : "=v"(r) : "v"(a), "v"(b), "v"(c)); return r; }
__device__ __forceinline__ float vmax2(float a, float b) { float r; asm("v_max_f32 %0, %1, %2" : "=v"(r) : "v"(a), "v"(b)); return r; }
__device__ __forceinline__ unsigned vnosent(float a) { unsigned r; asm("v_min_u32 %0, %1, %2" : "=v"(r) : "v"(a), "v"(0xFFFFFFFEu)); return r; }
__device__ __forceinline__ void swap16(float& a, float& b) { asm volatile("s_nop 1\n\tv_permlane16_swap_b32 %0, %1" : "+v"(a), "+v"(b)); }
__device__ __forceinline__ void swap32(float& a, float& b) { asm volatile("s_nop 1\n\tv_permlane32_swap_b32 %0, %1" : "+v"(a), "+v"(b)); }
__device__ __forceinline__ void pack_rows(const f32x4 a, const f32x4 b, unsigned& r0, unsigned& r1) {
    float t0, t1, t2, t3;
    asm("v_add_f32 %2, %6, %7\n\tv_add_f32 %3, %10, %11\n\tv_add_f32 %4, %8, %9\n\tv_add_f32 %5, %12, %13\n\t"
        "v_cvt_pk_bf16_f32 %0, %2, %3\n\tv_cvt_pk_bf16_f32 %1, %4, %5"
        : "=&v"(r0), "=&v"(r1), "=&v"(t0), "=&v"(t1), "=&v"(t2), "=&v"(t3)
        : "v"(a[0]), "v"(a[1]), "v"(a[2]), "v"(a[3]), "v"(b[0]), "v"(b[1]), "v"(b[2]), "v"(b[3]));
}
__device__ __forceinline__ void pack_cols(const f32x4 a, const f32x4 b, unsigned& r0, unsigned& r1) {
    float t0, t1, t2, t3;
    asm("v_add_f32 %2, %6, %7\n\tv_add_f32 %3, %8, %9\n\tv_add_f32 %4, %10, %11\n\tv_add_f32 %5, %12, %13\n\t"
        "v_cvt_pk_bf16_f32 %0, %2, %3\n\tv_cvt_pk_bf16_f32 %1, %4, %5"
        : "=&v"(r0), "=&v"(r1), "=&v"(t0), "=&v"(t1), "=&v"(t2), "=&v"(t3)
        : "v"(a[0]), "v"(a[1]), "v"(a[2]), "v"(a[3]), "v"(b[0]), "v"(b[1]), "v"(b[2]), "v"(b[3]));
}
__device__ __forceinline__ float neg_max_scaled(const f32x4 S0, const f32x4 S1, const f32x4 S2, float sc) {
    float m, t;
    asm("v_max3_f32 %0, %2, %3, %4\n\tv_max3_f32 %1, %5, %6, %7\n\tv_max3_f32 %0, %0, %8, %9\n\tv_max3_f32 %1, %1, %10, %11\n\tv_max_f32 %0, %0, %1\n\t"
        "v_mov_b32 %1, %0\n\ts_nop 1\n\tv_permlane16_swap_b32 %0, %1\n\tv_max_f32 %0, %0, %1\n\t"
        "v_mov_b32 %1, %0\n\ts_nop 1\n\tv_permlane32_swap_b32 %0, %1\n\tv_max_f32 %0, %0, %1\n\tv_mul_f32 %0, %0, %12"
        : "=&v"(m), "=&v"(t)
        : "v"(S1[0]), "v"(S1[1]), "v"(S1[2]), "v"(S1[3]), "v"(S2[0]), "v"(S2[1]), "v"(S0[0]), "v"(S0[1]), "v"(S0[2]), "v"(S0[3]), "v"(sc));
    return m;
}
__device__ __forceinline__ float recip_sum(const float (&p0)[4], const float (&p1)[4], const float (&p2)[2]) {
    float a, b, c;
    asm("v_add_f32 %0, %3, %4\n\tv_add_f32 %1, %5, %6\n\tv_add_f32 %2, %7, %8\n\tv_add_f32 %0, %0, %9\n\tv_add_f32 %1, %1, %10\n\tv_add_f32 %2, %2, %11\n\t"
        "v_add_f32 %0, %0, %12\n\tv_add_f32 %1, %1, %2\n\tv_add_f32 %0, %0, %1\n\t"
        "v_mov_b32 %1, %0\n\ts_nop 1\n\tv_permlane16_swap_b32 %0, %1\n\tv_add_f32 %0, %0, %1\n\t"
        "v_mov_b32 %1, %0\n\ts_nop 1\n\tv_permlane32_swap_b32 %0, %1\n\tv_add_f32 %0, %0, %1\n\tv_rcp_f32 %0, %0\n\ts_nop 0"
        : "=&v"(a), "=&v"(b), "=&v"(c)
        : "v"(p0[0]), "v"(p0[1]), "v"(p0[2]), "v"(p0[3]), "v"(p1[0]), "v"(p1[1]), "v"(p1[2]), "v"(p1[3]), "v"(p2[0]), "v"(p2[1]));
    return a;
}
__device__ __forceinline__ u32x4 scale_pack(const f32x4 a, const f32x4 b, float r) {
    u32x4 o; float t0, t1;
    asm("v_mul_f32 %4, %6, %14\n\tv_mul_f32 %5, %10, %14\n\tv_cvt_pk_bf16_f32 %0, %4, %5\n\t"
        "v_mul_f32 %4, %7, %14\n\tv_mul_f32 %5, %11, %14\n\tv_cvt_pk_bf16_f32 %1, %4, %5\n\t"
        "v_mul_f32 %4, %8, %14\n\tv_mul_f32 %5, %12, %14\n\tv_cvt_pk_bf16_f32 %2, %4, %5\n\t"
        "v_mul_f32 %4, %9, %14\n\tv_mul_f32 %5, %13, %14\n\tv_cvt_pk_bf16_f32 %3, %4, %5"
        : "=&v"(o.x), "=&v"(o.y), "=&v"(o.z), "=&v"(o.w), "=&v"(t0), "=&v"(t1)
        : "v"(a[0]), "v"(a[1]), "v"(a[2]), "v"(a[3]), "v"(b[0]), "v"(b[1]), "v"(b[2]), "v"(b[3]), "v"(r));
    return o;
}
__device__ __forceinline__ u32x4 part_pack(const f32x4 a, const f32x4 b, unsigned lim) {
    u32x4 o;
    asm("v_add_f32 %0, %4, %5\n\tv_add_f32 %1, %8, %9\n\tv_add_f32 %2, %6, %7\n\tv_add_f32 %3, %10, %11\n\t"
        "v_min_u32 %0, %0, %12\n\tv_min_u32 %1, %1, %12\n\tv_min_u32 %2, %2, %12\n\tv_min_u32 %3, %3, %12"
        : "=&v"(o.x), "=&v"(o.y), "=&v"(o.z), "=&v"(o.w)
        : "v"(a[0]), "v"(a[1]), "v"(a[2]), "v"(a[3]), "v"(b[0]), "v"(b[1]), "v"(b[2]), "v"(b[3]), "s"(lim));
    return o;
}
__device__ __forceinline__ float max8_x(const f32x4 a, const f32x4 b) {
    float m, t;
    asm("v_max3_f32 %0, %2, %3, %4\n\tv_max3_f32 %1, %5, %6, %7\n\tv_max3_f32 %0, %0, %8, %9\n\tv_max_f32 %0, %0, %1\n\t"
        "v_mov_b32 %1, %0\n\ts_nop 1\n\tv_permlane16_swap_b32 %0, %1\n\tv_max_f32 %0, %0, %1\n\t"
        "v_mov_b32 %1, %0\n\ts_nop 1\n\tv_permlane32_swap_b32 %0, %1\n\tv_max_f32 %0, %0, %1"
        : "=&v"(m), "=&v"(t)
        : "v"(a[0]), "v"(a[1]), "v"(a[2]), "v"(a[3]), "v"(b[0]), "v"(b[1]), "v"(b[2]), "v"(b[3]));
    return m;
}
__device__ __forceinline__ float max2_x(float a, float b, float mx) {
    float m, t;
    asm("v_max_f32 %0, %2, %3\n\t"
        "v_mov_b32 %1, %0\n\ts_nop 1\n\tv_permlane16_swap_b32 %0, %1\n\tv_max_f32 %0, %0, %1\n\t"
        "v_mov_b32 %1, %0\n\ts_nop 1\n\tv_permlane32_swap_b32 %0, %1\n\tv_max3_f32 %0, %0, %1, %4"
        : "=&v"(m), "=&v"(t) : "v"(a), "v"(b), "v"(mx));
    return m;
}
__device__ __forceinline__ float sum8_x(const float (&p)[4], const float (&q)[4]) {
    float a, b;
    asm("v_add_f32 %0, %2, %3\n\tv_add_f32 %1, %4, %5\n\tv_add_f32 %0, %0, %6\n\tv_add_f32 %1, %1, %7\n\tv_add_f32 %0, %0, %8\n\tv_add_f32 %1, %1, %9\n\tv_add_f32 %0, %0, %1\n\t"
        "v_mov_b32 %1, %0\n\ts_nop 1\n\tv_permlane16_swap_b32 %0, %1\n\tv_add_f32 %0, %0, %1\n\t"
        "v_mov_b32 %1, %0\n\ts_nop 1\n\tv_permlane32_swap_b32 %0, %1\n\tv_add_f32 %0, %0, %1"
        : "=&v"(a), "=&v"(b)
        : "v"(p[0]), "v"(p[1]), "v"(p[2]), "v"(p[3]), "v"(q[0]), "v"(q[1]), "v"(q[2]), "v"(q[3]));
    return a;
}
__device__ __forceinline__ float recip_l(float a, float b, float al, float lx) {
    float r, t;
    asm("v_add_f32 %0, %2, %3\n\t"
        "v_mov_b32 %1, %0\n\ts_nop 1\n\tv_permlane16_swap_b32 %0, %1\n\tv_add_f32 %0, %0, %1\n\t"
        "v_mov_b32 %1, %0\n\ts_nop 1\n\tv_permlane32_swap_b32 %0, %1\n\tv_add_f32 %0, %0, %1\n\tv_fma_f32 %0, %4, %5, %0\n\tv_rcp_f32 %0, %0\n\ts_nop 0"
        : "=&v"(r), "=&v"(t) : "v"(a), "v"(b), "v"(al), "v"(lx));
    return r;
}
__device__ __forceinline__ u32x4 pack_o(const f32x4 a, const f32x4 b) {
    u32x4 o;
    asm("v_cvt_pk_bf16_f32 %0, %4, %8\n\tv_cvt_pk_bf16_f32 %1, %5, %9\n\tv_cvt_pk_bf16_f32 %2, %6, %10\n\tv_cvt_pk_bf16_f32 %3, %7, %11"
        : "=&v"(o.x), "=&v"(o.y), "=&v"(o.z), "=&v"(o.w)
        : "v"(a[0]), "v"(a[1]), "v"(a[2]), "v"(a[3]), "v"(b[0]), "v"(b[1]), "v"(b[2]), "v"(b[3]));
    return o;
}
#define MF0(acc, A_, B_) asm volatile("v_mfma_f32_16x16x32_bf16 %0, %1, %2, 0" : "=&v"(acc) : "v"(A_), "v"(B_))
#define MFA(acc, A_, B_) asm volatile("v_mfma_f32_16x16x32_bf16 %0, %1, %2, %0" : "+v"(acc) : "v"(A_), "v"(B_))
struct ChainArgs {
    const bf16* x; bf16* out; const bf16* ax; const bf16* awqkv; const bf16* awo;
    const bf16* mem0; const bf16* Wq; const bf16* Wk; const bf16* Wv; const bf16* Wo;
    unsigned char* ws;
    int dA, dB, stop, pad;
};

__device__ __forceinline__ bool wait_flag(gu32* f, gu32* tmo) {
    for (unsigned spins = 0;; ++spins) {
        const unsigned v = __builtin_amdgcn_readfirstlane(__hip_atomic_load(f, RLX_AGENT));
        if (v != 0xFFFFFFFFu) return true;
        if (spins > SPIN_CAP) { __hip_atomic_store(tmo, 1u, RLX_AGENT); return false; }
        __builtin_amdgcn_s_sleep(PSLEEP);
    }
}

__device__ __forceinline__ void chain_role(const ChainArgs& a, const int b) {
    const int h = b >> 1, nh = b & 1;
    const int tid = threadIdx.x, lane = tid & 63, w = __builtin_amdgcn_readfirstlane(tid >> 6);
    const int g = lane >> 4, c = lane & 15;
    const __amdgpu_buffer_rsrc_t ws_r = __builtin_amdgcn_make_buffer_rsrc(a.ws, 0, 0x7fffffff, 0x00020000);
    gu32* flags = (gu32*)(a.ws + WS_FLAG);
    gu32* tmo = flags + FL_TMO;

#define STAMP(k)
#define LSTAMP(k)
#define CT(k)
    u32x8 W[3][2][8];
    u32x4 pend[2 * (8 - NVREG) * 2];
    {
        const int rj[8] = {0, 1, 8, 9, 2, 3, 10, 11};
#pragma unroll
        for (int M = 0; M < 3; ++M) {
            const bf16* Wm = (M == 0 ? a.Wq : (M == 1 ? a.Wk : a.Wv)) + h * 128 + 8 * c;
#pragma unroll
            for (int it = 0; it < 4; ++it) {
                const int df = 4 * w + it;
                const int rb = 32 * df + 16 * (g >> 1) + 4 * (g & 1);
                u32x4 r[8], f[8];
#pragma unroll
                for (int j = 0; j < 8; ++j) r[j] = *(const u32x4*)(Wm + (size_t)(rb + rj[j]) * 4096);
                tr8x8(r, f);
#pragma unroll
                for (int t = 0; t < 8; ++t) lds_wr16(L_STG + (unsigned)(((t * 16 + df) * 64 + lane) * 16), f[t]);
            }
            wg_barrier();
#pragma unroll
            for (int tau = 0; tau < 2; ++tau)
#pragma unroll
                for (int kk = 0; kk < 8; ++kk) {
                    const u32x4 lo = lds_rd16(L_STG + (unsigned)((((2 * w + tau) * 16 + 2 * kk) * 64 + lane) * 16));
                    const u32x4 hi = lds_rd16(L_STG + (unsigned)((((2 * w + tau) * 16 + 2 * kk + 1) * 64 + lane) * 16));
                    u32x8 v = {lo.x, lo.y, lo.z, lo.w, hi.x, hi.y, hi.z, hi.w};
                    W[M][tau][kk] = v;
                    if (M < 2) asm volatile("" : "+a"(W[M][tau][kk]));
                    else if (kk >= NVREG) {
                        pend[(tau * (8 - NVREG) + kk - NVREG) * 2] = lo; pend[(tau * (8 - NVREG) + kk - NVREG) * 2 + 1] = hi;
                    }
                }
            wg_barrier();
        }
#pragma unroll
        for (int q2 = 0; q2 < 2 * (8 - NVREG) * 2; ++q2) lds_wr16(L_WV + (unsigned)(((w * 2 * (8 - NVREG) * 2 + q2) * 64 + lane) * 16), pend[q2]);
        const int rj2[8] = {0, 1, 2, 3, 8, 9, 10, 11};
#pragma unroll
        for (int half = 0; half < 2; ++half) {
            const int m = 16 * half + c;
            const int rb = 32 * w + 16 * (g & 1) + 4 * (g >> 1);
            const bf16* Wp = a.Wo + (size_t)h * 128 * 512 + 256 * nh + 8 * m;
            u32x4 r[8], f[8];
#pragma unroll
            for (int j = 0; j < 8; ++j) r[j] = *(const u32x4*)(Wp + (size_t)(rb + rj2[j]) * 512);
            tr8x8(r, f);
#pragma unroll
            for (int t = 0; t < 8; ++t) {
                const int n = 8 * m + t;
                const int ww = n >> 6, n6 = n & 63, G4 = n6 >> 4, n4 = n6 & 15;
                const int tp = 2 * (G4 >> 1) + (n4 & 1), cc = 8 * (G4 & 1) + (n4 >> 1);
                lds_wr16(L_WO + (unsigned)((((ww * 4 + tp) * 4 + w) * 64 + g * 16 + cc) * 16), f[t]);
            }
        }
    }
#pragma unroll
    for (int q2 = 0; q2 < 2; ++q2) {
        const int kk = 2 * w + q2;
        const u32x4 v = *(const u32x4*)(a.mem0 + (c >> 1) * 512 + 64 * kk + 16 * g + 8 * (c & 1));
        lds_wr16(L_MEM + (unsigned)((kk * 64 + lane) * 16), v);
    }
    if (w == 1 || w == 2) {
        wait_flag(flags + FL_KV + (0 * 32 + h) * FS, tmo);
        const unsigned src = (unsigned)((w == 1 ? WS_KX : WS_VX) + (size_t)(0 * 32 + h) * 8192);
        const unsigned dst = (w == 1 ? L_KX : L_VX);
#pragma unroll
        for (int k = 0; k < 8; ++k) dma16(ws_r, src + k * 1024 + lane * 16, dst + k * 1024);
        asm volatile("s_waitcnt vmcnt(0)" ::: "memory");
    }
    if (tid == 0) lds_wr4(L_FLAG, 0);
    wg_barrier();

    const int idx = (c & 1) ? 0xEEEE : 0x4444;
    const float sc2 = 0.08838834764831845f * 1.4426950408889634f;
    unsigned epos = (unsigned)(((c >> 2) * 64 + (2 * (c & 1) + (w >> 1)) * 16 + 2 * g) * 16 + (4 * ((c >> 1) & 1) + 2 * (w & 1)) * 2);
    const int gp = c >> 2, rp = c & 3;
    unsigned qwo = L_Q + epos;
    unsigned l16 = (unsigned)lane * 16;
    unsigned a0o = 8192u + (unsigned)((g * 16 + 2 * gp + (rp & 1)) * 16);
    unsigned a2o = 4096u + (unsigned)((g * 16 + 4 * gp + 2 + (rp & 1)) * 16);
    unsigned qao = L_Q + (unsigned)((g * 16 + (c & 7)) * 16);
    unsigned wvb = L_WV + (unsigned)(w * 2 * (8 - NVREG) * 2 * 1024) + l16;
    unsigned wob = L_WO + (unsigned)(w * 16384) + l16;
    unsigned owo = (c < 8) ? L_O + (unsigned)(((g >> 1) * 64 + (2 * (g & 1) + (w >> 1)) * 16 + 2 * (c & 7) + (w & 1)) * 16) : L_DUMMY + (unsigned)tid * 16;
    unsigned pvo = (unsigned)((((2 * (16 * nh + 4 * w + (c >> 3)) + (g >> 1)) * 32 + h) * 2 + (g & 1)) * 128 + (c & 7) * 16);

    asm volatile("" : "+v"(l16), "+v"(a0o), "+v"(a2o), "+v"(qao), "+v"(wvb), "+v"(wob), "+v"(owo), "+v"(pvo), "+v"(epos), "+v"(qwo));
    f32x4 aq[2], ak[2], av[2];
    u32x4 pm0 = {0u, 0u, 0u, 0u}, pm1 = pm0, vmt = pm0;
#define ZACC() do { _Pragma("unroll") for (int t = 0; t < 2; ++t) { aq[t] = (f32x4){0, 0, 0, 0}; ak[t] = aq[t]; av[t] = aq[t]; } \
        asm volatile("" : "+v"(aq[0]), "+v"(aq[1]), "+v"(ak[0]), "+v"(ak[1]), "+v"(av[0]), "+v"(av[1])); } while (0)
    ZACC();
    for (int s = 0; s < 64; ++s) {
        const unsigned kxb = L_KX + (unsigned)(s & 1) * 12288, vxb = L_VX + (unsigned)(s & 1) * 8192;
        unsigned kvflag = 1u;
        if ((w == 1 || w == 2) && s < 63) kvflag = __hip_atomic_load(flags + FL_KV + ((s + 1) * 32 + h) * FS, RLX_AGENT);
        STAMP(0);
        CT(0);
#define SB() __builtin_amdgcn_sched_barrier(0)
        u32x4 A[8];
#pragma unroll
        for (int kk = 0; kk < 8; ++kk) A[kk] = lds_rd16(L_MEM + l16 + (unsigned)(kk * 1024));
        SB();
        const unsigned kxl = kxb + l16, k0l = kxb + a0o, vxl = vxb + (unsigned)(2 * w * 1024) + l16, kml = kxb + 8192 + epos;
        u32x4 ka[4], kb[4], vx[2], qf[4], k0[4];
        u32x8 wvl[2][8 - NVREG];
#pragma unroll
        for (int kk = 0; kk < 8; ++kk) {
            SMF_A(aq[0], A[kk], W[0][0][kk]); SMF_A(aq[1], A[kk], W[0][1][kk]);
            if (kk >= 2 && kk <= 5) { ka[kk - 2] = lds_rd16(kxl + (unsigned)((kk - 2) * 1024)); kb[kk - 2] = lds_rd16(kxl + 4096 + (unsigned)((kk - 2) * 1024)); SB(); }
        }
#pragma unroll
        for (int kk = 0; kk < 3; ++kk) { SMF_A(ak[0], A[kk], W[1][0][kk]); SMF_A(ak[1], A[kk], W[1][1][kk]); }
        asm volatile("" : "+v"(aq[0]), "+v"(aq[1]));
        { unsigned r0, r1; pack_rows(aq[0], aq[1], r0, r1); lds_wr4(qwo, r0); lds_wr4(qwo + 16, r1); }
        SB();
#pragma unroll
        for (int kk = 3; kk < 5; ++kk) { SMF_A(ak[0], A[kk], W[1][0][kk]); SMF_A(ak[1], A[kk], W[1][1][kk]); }
        CT(1);
        wg_barrier();
#pragma unroll
        for (int ks = 0; ks < 4; ++ks) qf[ks] = lds_rd16(qao + (unsigned)(ks * 1024));
        vx[0] = lds_rd16(vxl); vx[1] = lds_rd16(vxl + 1024);
        SB();
#pragma unroll
        for (int kk = 5; kk < 8; ++kk) { SMF_A(ak[0], A[kk], W[1][0][kk]); SMF_A(ak[1], A[kk], W[1][1][kk]); }
#pragma unroll
        for (int kk = 0; kk < 2; ++kk) { SMF_V(av[0], A[kk], W[2][0][kk]); SMF_V(av[1], A[kk], W[2][1][kk]); }
        f32x4 Sa, Sb, Sm;
        MF0(Sa, ka[0], qf[0]); MF0(Sb, kb[0], qf[0]); MFA(Sa, ka[1], qf[1]); MFA(Sb, kb[1], qf[1]);
        asm volatile("" : "+v"(ak[0]), "+v"(ak[1]));
        { unsigned r0, r1; pack_rows(ak[0], ak[1], r0, r1); lds_wr4(kml, r0); lds_wr4(kml + 16, r1); }
        SB();
        MFA(Sa, ka[2], qf[2]); MFA(Sb, kb[2], qf[2]); MFA(Sa, ka[3], qf[3]); MFA(Sb, kb[3], qf[3]);
        CT(2);
        wg_barrier();
        CT(3);
#pragma unroll
        for (int ks = 0; ks < 4; ++ks) k0[ks] = lds_rd16(k0l + (unsigned)(ks * 1024));
#pragma unroll
        for (int t = 0; t < 2; ++t)
#pragma unroll
            for (int kk = 0; kk < 8 - NVREG; ++kk) {
                const u32x4 lo = lds_rd16(wvb + (unsigned)(((t * (8 - NVREG) + kk) * 2) * 1024));
                const u32x4 hi = lds_rd16(wvb + (unsigned)(((t * (8 - NVREG) + kk) * 2 + 1) * 1024));
                wvl[t][kk] = (u32x8){lo.x, lo.y, lo.z, lo.w, hi.x, hi.y, hi.z, hi.w};
            }
        SB();
        SMF_V(av[0], A[2], W[2][0][2]); SMF_V(av[1], A[2], W[2][1][2]);
        asm volatile("" : "+v"(Sa), "+v"(Sb));
        const float mx = max8_x(Sa, Sb);
        SB();
        SMF_V(av[0], A[3], W[2][0][3]); SMF_V(av[1], A[3], W[2][1][3]);
        const float nmx = mx * -sc2;
        float pa[4], pb[4];
        pa[0] = __builtin_amdgcn_exp2f(__builtin_fmaf(Sa[0], sc2, nmx)); pa[1] = __builtin_amdgcn_exp2f(__builtin_fmaf(Sa[1], sc2, nmx));
        SB();
        SMF_V(av[0], A[4], W[2][0][4]);
        pa[2] = __builtin_amdgcn_exp2f(__builtin_fmaf(Sa[2], sc2, nmx)); pa[3] = __builtin_amdgcn_exp2f(__builtin_fmaf(Sa[3], sc2, nmx));
        SB();
        MF0(Sm, k0[0], qf[0]); MFA(Sm, k0[1], qf[1]);
        pb[0] = __builtin_amdgcn_exp2f(__builtin_fmaf(Sb[0], sc2, nmx)); pb[1] = __builtin_amdgcn_exp2f(__builtin_fmaf(Sb[1], sc2, nmx));
        SB();
        MFA(Sm, k0[2], qf[2]); MFA(Sm, k0[3], qf[3]);
        pb[2] = __builtin_amdgcn_exp2f(__builtin_fmaf(Sb[2], sc2, nmx)); pb[3] = __builtin_amdgcn_exp2f(__builtin_fmaf(Sb[3], sc2, nmx));
        SB();
        SMF_V(av[1], A[4], W[2][1][4]);
        u32x4 px = {pk2(pa[0], pa[1]), pk2(pa[2], pa[3]), pk2(pb[0], pb[1]), pk2(pb[2], pb[3])};
        SB();
        SMF_V(av[0], A[5], W[2][0][5]); SMF_V(av[1], A[5], W[2][1][5]);
        u32x8 wo[4][2];
#define LDWO(tp, kk2) do { const u32x4 lo = lds_rd16(wob + (unsigned)(((tp) * 4 + 2 * (kk2)) * 1024)); const u32x4 hi = lds_rd16(wob + (unsigned)(((tp) * 4 + 2 * (kk2) + 1) * 1024)); \
            wo[tp][kk2] = (u32x8){lo.x, lo.y, lo.z, lo.w, hi.x, hi.y, hi.z, hi.w}; } while (0)
        LDWO(0, 0); LDWO(1, 0); LDWO(2, 0);
        SB();
        CT(4);
        f32x4 o[2];
        asm volatile("s_nop 1" : "+v"(px), "+v"(Sm));
        const float m = max2_x(Sm[0], Sm[1], mx);
        MF0(o[0], vx[0], px); MF0(o[1], vx[1], px);
        SB();
        const float nm = m * -sc2;
        const float pm_0 = __builtin_amdgcn_exp2f(__builtin_fmaf(Sm[0], sc2, nm)), pm_1 = __builtin_amdgcn_exp2f(__builtin_fmaf(Sm[1], sc2, nm));
        const float al = __builtin_amdgcn_exp2f(__builtin_fmaf(mx, sc2, nm));
        SB();
        SMF_V(av[0], A[6], wvl[0][0]); SMF_V(av[1], A[6], wvl[1][0]);
        const float lx = sum8_x(pa, pb);
        SB();
        SMF_V(av[0], A[7], wvl[0][1]); SMF_V(av[1], A[7], wvl[1][1]);
        SB();
        LDWO(3, 0); LDWO(0, 1); LDWO(1, 1); LDWO(2, 1); LDWO(3, 1);
        SB();
        const float rl = recip_l(pm_0, pm_1, al, lx);
        const float als = al * rl;
        { const unsigned pmp = pk2(pm_0 * rl, pm_1 * rl); pm0.x = pmp; pm1.y = pmp; }
        asm volatile("" : "+v"(o[0]), "+v"(o[1]));
#pragma unroll
        for (int r = 0; r < 4; ++r) { o[0][r] *= als; o[1][r] *= als; }
        f32x4 pp[4];
#pragma unroll
        for (int tp = 0; tp < 4; ++tp) pp[tp] = (f32x4){0, 0, 0, 0};
        SB();
        asm volatile("" : "+v"(av[0]), "+v"(av[1]));
        { unsigned r0, r1; pack_cols(av[0], av[1], r0, r1); vmt.x = r0; vmt.y = r1; }
        CT(5);
        asm volatile("s_nop 1" : "+v"(pm0), "+v"(pm1), "+v"(vmt), "+v"(o[0]), "+v"(o[1]));
        MFA(o[0], vmt, pm0); MFA(o[1], vmt, pm1);
        lds_wr8(L_VM + (unsigned)(w * 512) + (l16 >> 1), (u32x2){vmt.x, vmt.y});
        asm volatile("s_nop 6" : "+v"(o[0]), "+v"(o[1]));
        lds_wr16(owo, pack_o(o[0], o[1]));
        CT(6);
        wg_barrier();
        CT(7);
        const u32x4 oa0 = lds_rd16(L_O + l16);
        const u32x4 oa1 = lds_rd16(L_O + 1024 + l16);
        asm volatile("s_nop 1" : "+v"(pp[0]), "+v"(pp[1]), "+v"(pp[2]), "+v"(pp[3]));
        SMF_V(pp[0], oa0, wo[0][0]); SMF_V(pp[1], oa0, wo[1][0]); SMF_V(pp[2], oa0, wo[2][0]); SMF_V(pp[3], oa0, wo[3][0]);
        SMF_V(pp[0], oa1, wo[0][1]); SMF_V(pp[1], oa1, wo[1][1]); SMF_V(pp[2], oa1, wo[2][1]); SMF_V(pp[3], oa1, wo[3][1]);
        asm volatile("s_nop 5" : "+v"(pp[0]), "+v"(pp[1]));
#pragma unroll
        for (int u = 0; u < 2; ++u) {
            if (u == 1) asm volatile("" : "+v"(pp[2]), "+v"(pp[3]));
            const u32x4 v = part_pack(pp[2 * u], pp[2 * u + 1], 0xFFFFFFFEu);
            __builtin_amdgcn_raw_buffer_store_b128(v, ws_r, pvo + (unsigned)(u * 4 * 8192), (unsigned)(WS_PART + (size_t)(s % RING) * 64 * 8192), 16);
        }
        SB();
        CT(8);
        STAMP(1);
        LSTAMP(0);
        int ln = lane; asm volatile("" : "+v"(ln));
        if (w == 3) {
            if (nh == 0) {
#pragma unroll
                for (int q2 = 0; q2 < 2; ++q2) {
                    const int ch = q2 * 64 + ln;
                    const u32x4 v = lds_rd16(kxb + 8192 + (unsigned)((((ch >> 5) * 64) + ((ch >> 3) & 3) * 16 + (ch & 7)) * 16));
                    __builtin_amdgcn_raw_buffer_store_b128(v, ws_r, (unsigned)(WS_KM + (size_t)((s % KVR) * 32 + h) * 2048) + ch * 16, 0, 16);
                }
            } else {
#pragma unroll
                for (int q2 = 0; q2 < 2; ++q2) {
                    const u32x4 v = lds_rd16(L_VM + (unsigned)((q2 * 64 + ln) * 16));
                    __builtin_amdgcn_raw_buffer_store_b128(v, ws_r, (unsigned)(WS_VM + (size_t)((s % KVR) * 32 + h) * 2048) + (q2 * 64 + ln) * 16, 0, 16);
                }
            }
            asm volatile("s_waitcnt vmcnt(0)" ::: "memory");
            if (ln == 0) __hip_atomic_store(flags + (nh == 0 ? FL_KM : FL_VM) + (s * 32 + h) * FS, 1u, RLX_AGENT);
        }
        if (s == 63) break;
        if (w == 1 || w == 2) {
            if (__builtin_amdgcn_readfirstlane(kvflag) == 0xFFFFFFFFu) wait_flag(flags + FL_KV + ((s + 1) * 32 + h) * FS, tmo);
            const unsigned src = (unsigned)((w == 1 ? WS_KX : WS_VX) + (size_t)(((s + 1) % KVR) * 32 + h) * 8192);
            const unsigned dst = (w == 1 ? L_KX + (unsigned)((s + 1) & 1) * 12288 : L_VX + (unsigned)((s + 1) & 1) * 8192);
#pragma unroll
            for (int k = 0; k < 8; ++k) dma16(ws_r, src + k * 1024 + ln * 16, dst + k * 1024);
        }
        if (w == 0) {
            for (int d = 0; d < a.dA; ++d) __builtin_amdgcn_s_sleep(1);
            const unsigned pbase = (unsigned)(WS_PART + (size_t)(s % RING) * 64 * 8192) + (unsigned)b * 8192;
            const unsigned loff = (unsigned)((((2 * (ln >> 5) + ((ln >> 3) & 1)) * 2 + ((ln >> 4) & 1)) * 8 + (ln & 7)) * 16);
            u32x4 p[8];
            STAMP(5);
            for (unsigned spins = 0;; ++spins) {
                asm volatile("" ::: "memory");
#pragma unroll
                for (int k = 0; k < 8; ++k) p[k] = __builtin_amdgcn_raw_buffer_load_b128(ws_r, pbase + k * 1024 + loff, 0, 16);
                unsigned mx = 0;
#pragma unroll
                for (int k = 0; k < 8; ++k) mx = max(mx, max4(p[k]));
                if (__all(mx != ~0u)) break;
                if (spins > SPIN_CAP) { __hip_atomic_store(tmo, 2u, RLX_AGENT); break; }
            }
            STAMP(2);
            LSTAMP(1);
            float f[4] = {0, 0, 0, 0};
#pragma unroll
            for (int k = 0; k < 8; ++k)
#pragma unroll
                for (int e = 0; e < 4; ++e) f[e] += __uint_as_float((unsigned)p[k][e]);
#pragma unroll
            for (int e = 0; e < 4; ++e) { f[e] += xor8f(f[e]); f[e] += xor32f(f[e]); }
            unsigned d0 = pk2(f[0], f[1]), d1 = pk2(f[2], f[3]);
            unsigned dv = (ln & 8) ? d1 : d0;
            dv = dv == ~0u ? 0x7fc07fc0u : dv;
            if (ln < 32) __builtin_amdgcn_raw_buffer_store_b32(dv, ws_r, (unsigned)(WS_MEMS + (size_t)(s + 1) * 8192) + (unsigned)(b * 128 + ln * 4), 0, 16);
            if (ln == 0) lds_wr4(L_FLAG, (unsigned)(s + 1));
            STAMP(3);
            LSTAMP(2);
            if (s == a.stop) return;
            const u32x4 pz = {~0u, ~0u, ~0u, ~0u};
#pragma unroll
            for (int k = 0; k < 8; ++k) __builtin_amdgcn_raw_buffer_store_b128(pz, ws_r, pbase + k * 1024 + ln * 16, 0, 16);
        } else {
            if (s == a.stop) return;
            for (unsigned spins = 0; lds_rd4(L_FLAG) != (unsigned)(s + 1); ++spins) { asm volatile("" ::: "memory"); __builtin_amdgcn_s_sleep(1); if (spins > SPIN_CAP) break; }
        }
        ZACC();
        {
            for (int d = 0; d < a.dB; ++d) __builtin_amdgcn_s_sleep(1);
            const unsigned mo = (unsigned)(WS_MEMS + (size_t)(s + 1) * 8192) + (unsigned)(w * 2048 + ln * 16);
            u32x4 m0, m1;
            for (unsigned spins = 0;; ++spins) {
                asm volatile("" ::: "memory");
                m0 = __builtin_amdgcn_raw_buffer_load_b128(ws_r, mo, 0, 16);
                m1 = __builtin_amdgcn_raw_buffer_load_b128(ws_r, mo + 1024, 0, 16);
                if (__all(max(max4(m0), max4(m1)) != ~0u)) break;
                if (spins > SPIN_CAP) { __hip_atomic_store(tmo, 3u, RLX_AGENT); break; }
            }
            STAMP(4);
            LSTAMP(3);
            lds_wr16(L_MEM + (unsigned)(w * 2048 + ln * 16), m0);
            lds_wr16(L_MEM + (unsigned)(w * 2048 + 1024 + ln * 16), m1);
            asm volatile("s_waitcnt vmcnt(0)" ::: "memory");
            wg_barrier();
        }
    }
}

template <int NATURAL, int PIN, int KMODE = 0>
__device__ __forceinline__ void load_panel(const bf16* Wp, const int ld, u32x4 (&F)[2][16], const int w, const int lane) {
    const int g = lane >> 4, c = lane & 15;
#pragma unroll
    for (int it = 0; it < 4; ++it) {
        const int kk = 4 * w + it;
        u32x4 r[8], f[8];
#pragma unroll
        for (int j = 0; j < 8; ++j) r[j] = *(const u32x4*)(Wp + (size_t)(KMODE ? (32 * kk + 16 * (j >> 2) + 4 * g + (j & 3)) : (32 * kk + 8 * g + j)) * ld + 8 * c);
        tr8x8(r, f);
#pragma unroll
        for (int t = 0; t < 8; ++t) lds_wr16((unsigned)((t * 1026 + kk * 64 + lane) * 16), f[t]);
    }
    wg_barrier();
#pragma unroll
    for (int tau = 0; tau < 2; ++tau)
#pragma unroll
        for (int kk = 0; kk < 16; ++kk) {
            const int T = 2 * w + tau;
            const unsigned ad = NATURAL ? (unsigned)(((c & 7) * 1026 + kk * 64 + g * 16 + 2 * T + (c >> 3)) * 16) : (unsigned)((T * 1026 + kk * 64 + lane) * 16);
            F[tau][kk] = lds_rd16(ad);
            if (PIN) asm volatile("" : "+a"(F[tau][kk]));
        }
    wg_barrier();
}
#define MFD(acc, A_, B_, CA, CB) asm volatile("v_mfma_f32_16x16x32_bf16 %0, %1, %2, %0" : "+v"(acc) : CA(A_), CB(B_))

constexpr unsigned H_X = 0;
constexpr unsigned H_XSZ = 32 * 1040;
constexpr unsigned H_ST = 2 * H_XSZ;
constexpr unsigned H_KM = H_ST + 3 * 24576;
constexpr unsigned H_VM = H_KM + 4096;

__device__ __forceinline__ void head_role(const ChainArgs& a, const int u) {
    const int h = u >> 1, par = u & 1;
    const int tid = threadIdx.x, lane = tid & 63, w = __builtin_amdgcn_readfirstlane(tid >> 6);
    const int g = lane >> 4, c = lane & 15;
    const __amdgpu_buffer_rsrc_t ws_r = __builtin_amdgcn_make_buffer_rsrc(a.ws, 0, 0x7fffffff, 0x00020000);
    const __amdgpu_buffer_rsrc_t x_r = __builtin_amdgcn_make_buffer_rsrc((void*)a.x, 0, 2048 * 512 * 2, 0x00020000);
    gu32* flags = (gu32*)(a.ws + WS_FLAG);
    gu32* tmo = flags + FL_TMO;
    u32x4 FK[2][16], FV[2][16], FQ[2][16];
    load_panel<1, 1>(a.Wk + h * 128, 4096, FK, w, lane);
    load_panel<0, 0>(a.Wv + h * 128, 4096, FV, w, lane);
    load_panel<1, 1>(a.Wq + h * 128, 4096, FQ, w, lane);
    const float sc2 = 0.08838834764831845f * 1.4426950408889634f;
    auto load_x = [&](int s, int buf) {
#pragma unroll
        for (int r = 0; r < 8; ++r) {
            unsigned keep;
            const unsigned voff = (unsigned)(((s * 32 + 8 * w + r) * 512) * 2 + lane * 16), dst = H_X + (unsigned)buf * H_XSZ + (unsigned)(8 * w + r) * 1040;
            asm volatile("s_mov_b32 %0, m0\n\ts_mov_b32 m0, %3\n\ts_nop 0\n\tbuffer_load_dwordx4 %1, %2, 0 offen lds\n\ts_mov_b32 m0, %0" : "=&s"(keep) : "v"(voff), "s"(x_r), "s"(dst) : "memory");
        }
    };
    load_x(par, 0);
    for (int i = 0; i < 32 + 2; ++i) {
        if (i < 32) {
            const int s = 2 * i + par;
            asm volatile("s_waitcnt vmcnt(0)" ::: "memory");
            wg_barrier();
            if (i + 1 < 32) load_x(s + 2, (i + 1) & 1);
            const unsigned xb = H_X + (unsigned)(i & 1) * H_XSZ + (unsigned)(c * 1040 + g * 16);
            f32x4 dk[2][2], dq[2][2], dv[2][2];
#pragma unroll
            for (int t = 0; t < 2; ++t)
#pragma unroll
                for (int tt = 0; tt < 2; ++tt) { dk[t][tt] = (f32x4){0, 0, 0, 0}; dq[t][tt] = dk[t][tt]; dv[t][tt] = dk[t][tt]; }
            asm volatile("s_nop 1" : "+v"(dk[0][0]), "+v"(dk[0][1]), "+v"(dk[1][0]), "+v"(dk[1][1]), "+v"(dq[0][0]), "+v"(dq[0][1]), "+v"(dq[1][0]), "+v"(dq[1][1]));
            asm volatile("s_nop 1" : "+v"(dv[0][0]), "+v"(dv[0][1]), "+v"(dv[1][0]), "+v"(dv[1][1]));
#pragma unroll
            for (int kk = 0; kk < 16; ++kk) {
                const u32x4 x0 = lds_rd16(xb + (unsigned)(kk * 64)), x1 = lds_rd16(xb + (unsigned)(16 * 1040 + kk * 64));
#pragma unroll
                for (int t = 0; t < 2; ++t) {
                    MFD(dk[t][0], FK[t][kk], x0, "a", "v"); MFD(dk[t][1], FK[t][kk], x1, "a", "v");
                    MFD(dv[t][0], x0, FV[t][kk], "v", "v"); MFD(dv[t][1], x1, FV[t][kk], "v", "v");
                    MFD(dq[t][0], FQ[t][kk], x0, "a", "v"); MFD(dq[t][1], FQ[t][kk], x1, "a", "v");
                }
            }
            asm volatile("s_nop 7" : "+v"(dk[0][0]), "+v"(dk[0][1]), "+v"(dk[1][0]), "+v"(dk[1][1]), "+v"(dq[0][0]), "+v"(dq[0][1]), "+v"(dq[1][0]), "+v"(dq[1][1]));
            asm volatile("" : "+v"(dv[0][0]), "+v"(dv[0][1]), "+v"(dv[1][0]), "+v"(dv[1][1]));
            const unsigned st = H_ST + (unsigned)(i % 3) * 24576;
            const unsigned gk = (unsigned)(WS_KX + (size_t)((s % KVR) * 32 + h) * 8192), gv = (unsigned)(WS_VX + (size_t)((s % KVR) * 32 + h) * 8192);
#pragma unroll
            for (int tt = 0; tt < 2; ++tt) {
                const u32x4 kf = {pk2(dk[0][tt][0], dk[0][tt][1]), pk2(dk[0][tt][2], dk[0][tt][3]), pk2(dk[1][tt][0], dk[1][tt][1]), pk2(dk[1][tt][2], dk[1][tt][3])};
                const u32x4 qf = {pk2(dq[0][tt][0], dq[0][tt][1]), pk2(dq[0][tt][2], dq[0][tt][3]), pk2(dq[1][tt][0], dq[1][tt][1]), pk2(dq[1][tt][2], dq[1][tt][3])};
                const unsigned o = (unsigned)((tt * 4 + w) * 1024 + lane * 16);
                __builtin_amdgcn_raw_buffer_store_b128(kf, ws_r, gk + o, 0, 16);
                lds_wr16(st + o, qf); lds_wr16(st + 8192 + o, kf);
            }
#pragma unroll
            for (int t = 0; t < 2; ++t) {
                const u32x4 vf = {pk2(dv[t][0][0], dv[t][0][1]), pk2(dv[t][0][2], dv[t][0][3]), pk2(dv[t][1][0], dv[t][1][1]), pk2(dv[t][1][2], dv[t][1][3])};
                const unsigned o = (unsigned)((2 * w + t) * 1024 + lane * 16);
                __builtin_amdgcn_raw_buffer_store_b128(vf, ws_r, gv + o, 0, 16);
                lds_wr16(st + 16384 + o, vf);
            }
            asm volatile("s_waitcnt vmcnt(0)" ::: "memory");
            wg_barrier();
            if (tid == 0) __hip_atomic_store(flags + FL_KV + (s * 32 + h) * FS, 1u, RLX_AGENT);
        }
        if (i >= 2 && a.pad == 0) {
            const int s = 2 * (i - 2) + par;
            const unsigned st = H_ST + (unsigned)((i - 2) % 3) * 24576;
            if (w == 0) {
                wait_flag(flags + FL_KM + (s * 32 + h) * FS, tmo);
#pragma unroll
                for (int q2 = 0; q2 < 2; ++q2) {
                    const int ch = q2 * 64 + lane;
                    const u32x4 v = __builtin_amdgcn_raw_buffer_load_b128(ws_r, (unsigned)(WS_KM + (size_t)((s % KVR) * 32 + h) * 2048) + ch * 16, 0, 16);
                    lds_wr16(H_KM + (unsigned)((((ch >> 5) * 64) + ((ch >> 3) & 3) * 16 + (ch & 7)) * 16), v);
                }
            } else if (w == 1) {
                wait_flag(flags + FL_VM + (s * 32 + h) * FS, tmo);
#pragma unroll
                for (int q2 = 0; q2 < 2; ++q2) {
                    const u32x4 v = __builtin_amdgcn_raw_buffer_load_b128(ws_r, (unsigned)(WS_VM + (size_t)((s % KVR) * 32 + h) * 2048) + (q2 * 64 + lane) * 16, 0, 16);
                    lds_wr16(H_VM + (unsigned)((q2 * 64 + lane) * 16), v);
                }
            }
            wg_barrier();
            const int qt = w & 1, eh = w >> 1;
            f32x4 Sm, Sa, Sb;
            const unsigned km = H_KM + (unsigned)((g * 16 + 2 * (c >> 2) + (c & 1)) * 16);
            {
                u32x4 q8[4], kmf[4], kaf[4], kbf[4];
#pragma unroll
                for (int ks = 0; ks < 4; ++ks) {
                    q8[ks] = lds_rd16(st + (unsigned)((qt * 4 + ks) * 1024 + lane * 16));
                    kmf[ks] = lds_rd16(km + ks * 1024);
                    kaf[ks] = lds_rd16(st + 8192 + (unsigned)(ks * 1024 + lane * 16));
                    kbf[ks] = lds_rd16(st + 8192 + (unsigned)((4 + ks) * 1024 + lane * 16));
                }
                MF0(Sm, kmf[0], q8[0]); MF0(Sa, kaf[0], q8[0]); MF0(Sb, kbf[0], q8[0]);
#pragma unroll
                for (int ks = 1; ks < 4; ++ks) { MFA(Sm, kmf[ks], q8[ks]); MFA(Sa, kaf[ks], q8[ks]); MFA(Sb, kbf[ks], q8[ks]); }
                asm volatile("s_nop 7" : "+v"(Sm), "+v"(Sa), "+v"(Sb));
            }
            const float NEG = -3.0e38f;
            float sa[4], sb[4];
#pragma unroll
            for (int r = 0; r < 4; ++r) {
                const bool diag = (4 * g + r) <= c;
                sa[r] = (qt == 1 || diag) ? Sa[r] : NEG;
                sb[r] = (qt == 1 && diag) ? Sb[r] : NEG;
            }
            float m = fmaxf(fmaxf(fmaxf(sa[0], sa[1]), fmaxf(sa[2], sa[3])), fmaxf(fmaxf(sb[0], sb[1]), fmaxf(sb[2], sb[3])));
            m = fmaxf(m, fmaxf(Sm[0], Sm[1]));
            m = fmaxf(m, xor16f(m)); m = fmaxf(m, xor32f(m));
            const float nm = -m * sc2;
            float pa[4], pb[4];
#pragma unroll
            for (int r = 0; r < 4; ++r) {
                pa[r] = sa[r] == NEG ? 0.f : __builtin_amdgcn_exp2f(__builtin_fmaf(sa[r], sc2, nm));
                pb[r] = sb[r] == NEG ? 0.f : __builtin_amdgcn_exp2f(__builtin_fmaf(sb[r], sc2, nm));
            }
            const float pm0 = __builtin_amdgcn_exp2f(__builtin_fmaf(Sm[0], sc2, nm)), pm1 = __builtin_amdgcn_exp2f(__builtin_fmaf(Sm[1], sc2, nm));
            float l = ((pa[0] + pa[1]) + (pa[2] + pa[3])) + ((pb[0] + pb[1]) + (pb[2] + pb[3])) + (pm0 + pm1);
            l += xor16f(l); l += xor32f(l);
            const float rl = __builtin_amdgcn_rcpf(l);
            u32x4 px = {pk2(pa[0] * rl, pa[1] * rl), pk2(pa[2] * rl, pa[3] * rl), pk2(pb[0] * rl, pb[1] * rl), pk2(pb[2] * rl, pb[3] * rl)};
            u32x4 pmv = {pk2(pm0 * rl, pm1 * rl), 0u, 0u, 0u};
            f32x4 o[4];
#pragma unroll
            for (int tq = 0; tq < 4; ++tq) {
                const int t = 4 * eh + tq;
                const u32x4 vx = lds_rd16(st + 16384 + (unsigned)(t * 1024 + lane * 16));
                const unsigned vm = lds_rd4(H_VM + (unsigned)(((t >> 1) * 64 + lane) * 8 + (t & 1) * 4));
                u32x4 va = {vm, 0u, 0u, 0u};
                asm volatile("s_nop 1" : "+v"(va), "+v"(px), "+v"(pmv));
                MF0(o[tq], vx, px);
                MFA(o[tq], va, pmv);
            }
            asm volatile("s_nop 7" : "+v"(o[0]), "+v"(o[1]), "+v"(o[2]), "+v"(o[3]));
            const unsigned go = (unsigned)(WS_OX + (size_t)((s * 2 + qt) * 128 + h * 4 + eh * 2) * 1024) + (unsigned)lane * 16;
#pragma unroll
            for (int f = 0; f < 2; ++f) {
                const u32x4 v = {pk2(o[0][2 * f], o[1][2 * f]), pk2(o[2][2 * f], o[3][2 * f]), pk2(o[0][2 * f + 1], o[1][2 * f + 1]), pk2(o[2][2 * f + 1], o[3][2 * f + 1])};
                __builtin_amdgcn_raw_buffer_store_b128(v, ws_r, go + f * 1024, 0, 16);
            }
            asm volatile("s_waitcnt vmcnt(0)" ::: "memory");
            wg_barrier();
            if (tid == 0) __hip_atomic_fetch_add(flags + FL_O + s * 32, 1u, RLX_AGENT);
        }
    }
}

constexpr int NREP = 3;
__device__ __forceinline__ void oproj_role(const ChainArgs& a, const int u) {
    const int n = u & 15, rp = u >> 4;
    const int tid = threadIdx.x, lane = tid & 63, w = __builtin_amdgcn_readfirstlane(tid >> 6);
    const int g = lane >> 4, c = lane & 15;
    const __amdgpu_buffer_rsrc_t ws_r = __builtin_amdgcn_make_buffer_rsrc(a.ws, 0, 0x7fffffff, 0x00020000);
    gu32* flags = (gu32*)(a.ws + WS_FLAG);
    gu32* tmo = flags + FL_TMO;
    u32x4 F[32][2];
    {
        const int kq = lane >> 4, gg = (lane >> 2) & 3, oct = lane & 3;
        const unsigned lb = (unsigned)w * 16384;
#pragma unroll
        for (int it = 0; it < 8; ++it) {
            const int kk = 32 * w + 4 * it + kq;
            const int hh = kk >> 2, eh = (kk >> 1) & 1, f = kk & 1;
            const bf16* Wp = a.Wo + (size_t)(hh * 128 + 32 * gg + 16 * f + 4 * eh) * 512 + 32 * n + 8 * oct;
            u32x4 r[8], fr[8];
#pragma unroll
            for (int j = 0; j < 8; ++j) r[j] = *(const u32x4*)(Wp + (size_t)(8 * (j >> 2) + (j & 3)) * 512);
            tr8x8(r, fr);
#pragma unroll
            for (int t = 0; t < 8; ++t) lds_wr16(lb + (unsigned)((((kq * 2 + (oct >> 1)) * 4 + gg) * 16 + 8 * (oct & 1) + t) * 16), fr[t]);
            asm volatile("s_waitcnt lgkmcnt(0)" ::: "memory");
#pragma unroll
            for (int q4 = 0; q4 < 4; ++q4)
#pragma unroll
                for (int T = 0; T < 2; ++T) { F[4 * it + q4][T] = lds_rd16(lb + (unsigned)(((q4 * 2 + T) * 64 + lane) * 16)); asm volatile("" : "+a"(F[4 * it + q4][T])); }
            asm volatile("s_waitcnt lgkmcnt(0)" ::: "memory");
        }
    }
    wg_barrier();
    for (int un = rp; un < 128; un += NREP) {
        const int s = un >> 1, rh = un & 1;
        if (w == 0) {
            gu32* f = flags + FL_O + s * 32;
            for (unsigned spins = 0;; ++spins) {
                const unsigned v = __builtin_amdgcn_readfirstlane(__hip_atomic_load(f, RLX_AGENT));
                if (v == 31u) break;
                if (spins > SPIN_CAP) { __hip_atomic_store(tmo, 4u, RLX_AGENT); break; }
                __builtin_amdgcn_s_sleep(PSLEEP * 2);
            }
        }
        wg_barrier();
        const unsigned ab = (unsigned)(WS_OX + (size_t)((s * 2 + rh) * 128 + 32 * w) * 1024) + (unsigned)lane * 16;
        f32x4 d0 = {0, 0, 0, 0}, d1 = d0;
        asm volatile("s_nop 1" : "+v"(d0), "+v"(d1));
#pragma unroll
        for (int hb = 0; hb < 2; ++hb) {
            u32x4 A[16];
#pragma unroll
            for (int i = 0; i < 16; ++i) A[i] = __builtin_amdgcn_raw_buffer_load_b128(ws_r, ab + (unsigned)((16 * hb + i) * 1024), 0, 16);
#pragma unroll
            for (int i = 0; i < 16; ++i) { MFD(d0, A[i], F[16 * hb + i][0], "v", "a"); MFD(d1, A[i], F[16 * hb + i][1], "v", "a"); }
        }
        asm volatile("s_nop 7" : "+v"(d0), "+v"(d1));
        lds_wr16(65536u + (unsigned)(((w * 2 + 0) * 64 + lane) * 16), __builtin_bit_cast(u32x4, d0));
        lds_wr16(65536u + (unsigned)(((w * 2 + 1) * 64 + lane) * 16), __builtin_bit_cast(u32x4, d1));
        wg_barrier();
        {
            const int tok = tid >> 4, cp = tid & 15, T = cp >> 3, c0 = (2 * cp) & 15;
            float v0 = 0, v1 = 0;
#pragma unroll
            for (int ww = 0; ww < 4; ++ww) {
                const unsigned ad = 65536u + (unsigned)((((ww * 2 + T) * 64) + (tok >> 2) * 16 + c0) * 16 + (tok & 3) * 4);
                v0 += __uint_as_float(lds_rd4(ad)); v1 += __uint_as_float(lds_rd4(ad + 16));
            }
            *(unsigned*)(a.out + (size_t)(s * 32 + rh * 16 + tok) * 1024 + 512 + 32 * n + 2 * cp) = pk2(v0, v1);
        }
        wg_barrier();
    }
}

__device__ __forceinline__ void wait_count(gu32* f, unsigned want, gu32* tmo) {
    for (unsigned spins = 0;; ++spins) {
        const unsigned v = __builtin_amdgcn_readfirstlane(__hip_atomic_load(f, RLX_AGENT));
        if (v == want) return;
        if (spins > SPIN_CAP) { __hip_atomic_store(tmo, 5u, RLX_AGENT); return; }
        __builtin_amdgcn_s_sleep(8);
    }
}
template <int NV>
__device__ __forceinline__ void a1_unit(const ChainArgs& a, const int j, const int rg) {
    const int tid = threadIdx.x, lane = tid & 63, w = __builtin_amdgcn_readfirstlane(tid >> 6);
    const int g = lane >> 4, c = lane & 15;
    const __amdgpu_buffer_rsrc_t ws_r = __builtin_amdgcn_make_buffer_rsrc(a.ws, 0, 0x7fffffff, 0x00020000);
    const __amdgpu_buffer_rsrc_t x_r = __builtin_amdgcn_make_buffer_rsrc((void*)a.ax, 0, 2048 * 512 * 2, 0x00020000);
    u32x4 F0[2][16], F1[2][16], F2[2][16];
    load_panel<1, 1>(a.awqkv + 384 * j, 1536, F0, w, lane);
    load_panel<1, 1>(a.awqkv + 384 * j + 128, 1536, F1, w, lane);
    load_panel<1, 0>(a.awqkv + 384 * j + 256, 1536, F2, w, lane);
    auto load_x = [&](int t32, int buf) {
#pragma unroll
        for (int r = 0; r < 8; ++r) {
            unsigned keep;
            const unsigned voff = (unsigned)(((t32 * 32 + 8 * w + r) * 512) * 2 + lane * 16), dst = H_X + (unsigned)buf * H_XSZ + (unsigned)(8 * w + r) * 1040;
            asm volatile("s_mov_b32 %0, m0\n\ts_mov_b32 m0, %3\n\ts_nop 0\n\tbuffer_load_dwordx4 %1, %2, 0 offen lds\n\ts_mov_b32 m0, %0" : "=&s"(keep) : "v"(voff), "s"(x_r), "s"(dst) : "memory");
        }
    };
    load_x(rg * 4, 0);
    for (int i = 0; i < 4; ++i) {
        asm volatile("s_waitcnt vmcnt(0)" ::: "memory");
        wg_barrier();
        if (i + 1 < 4) load_x(rg * 4 + i + 1, (i + 1) & 1);
        const unsigned xb = H_X + (unsigned)(i & 1) * H_XSZ + (unsigned)(c * 1040 + g * 16);
        f32x4 d[3][2][2];
#pragma unroll
        for (int p = 0; p < 3; ++p)
#pragma unroll
            for (int t = 0; t < 2; ++t) { d[p][t][0] = (f32x4){0, 0, 0, 0}; d[p][t][1] = d[p][t][0]; }
        asm volatile("s_nop 1" : "+v"(d[0][0][0]), "+v"(d[0][0][1]), "+v"(d[0][1][0]), "+v"(d[0][1][1]), "+v"(d[1][0][0]), "+v"(d[1][0][1]), "+v"(d[1][1][0]), "+v"(d[1][1][1]));
        asm volatile("s_nop 1" : "+v"(d[2][0][0]), "+v"(d[2][0][1]), "+v"(d[2][1][0]), "+v"(d[2][1][1]));
#pragma unroll
        for (int kk = 0; kk < 16; ++kk) {
            const u32x4 x0 = lds_rd16(xb + (unsigned)(kk * 64)), x1 = lds_rd16(xb + (unsigned)(16 * 1040 + kk * 64));
#pragma unroll
            for (int t = 0; t < 2; ++t) {
                if (NV >= 3) { MFD(d[0][t][0], x0, F0[t][kk], "v", "a"); MFD(d[0][t][1], x1, F0[t][kk], "v", "a"); MFD(d[1][t][0], x0, F1[t][kk], "v", "a"); MFD(d[1][t][1], x1, F1[t][kk], "v", "a"); }
                else { MFD(d[0][t][0], F0[t][kk], x0, "a", "v"); MFD(d[0][t][1], F0[t][kk], x1, "a", "v"); MFD(d[1][t][0], F1[t][kk], x0, "a", "v"); MFD(d[1][t][1], F1[t][kk], x1, "a", "v"); }
                if (NV >= 1) { MFD(d[2][t][0], x0, F2[t][kk], "v", "v"); MFD(d[2][t][1], x1, F2[t][kk], "v", "v"); }
                else { MFD(d[2][t][0], F2[t][kk], x0, "v", "v"); MFD(d[2][t][1], F2[t][kk], x1, "v", "v"); }
            }
        }
        asm volatile("s_nop 7" : "+v"(d[0][0][0]), "+v"(d[0][0][1]), "+v"(d[0][1][0]), "+v"(d[0][1][1]), "+v"(d[1][0][0]), "+v"(d[1][0][1]), "+v"(d[1][1][0]), "+v"(d[1][1][1]));
        asm volatile("" : "+v"(d[2][0][0]), "+v"(d[2][0][1]), "+v"(d[2][1][0]), "+v"(d[2][1][1]));
#pragma unroll
        for (int p = 0; p < 3; ++p)
#pragma unroll
            for (int t = 0; t < 2; ++t)
#pragma unroll
                for (int tt = 0; tt < 2; ++tt) {
                    const int hd = (384 * j + 128 * p) / 16 + 2 * w + t;
                    const int tile = (rg * 4 + i) * 2 + tt;
                    const u32x2 v = {pk2(d[p][t][tt][0], d[p][t][tt][1]), pk2(d[p][t][tt][2], d[p][t][tt][3])};
                    __builtin_amdgcn_raw_buffer_store_b64(v, ws_r, (unsigned)(WS_AQ + (size_t)(hd * 128 + tile) * 512) + (unsigned)lane * 8, 0, 16);
                }
    }
    asm volatile("s_waitcnt vmcnt(0)" ::: "memory");
    wg_barrier();
    if (tid == 0) __hip_atomic_fetch_add((gu32*)(a.ws + WS_FLAG) + FL_A1 + rg * 32, 1u, RLX_AGENT);
}

__device__ __forceinline__ void a2_unit(const ChainArgs& a, const int un) {
    const int rg64 = un >> 2, ho = un & 3;
    const int tid = threadIdx.x, lane = tid & 63, w = __builtin_amdgcn_readfirstlane(tid >> 6);
    const int g = lane >> 4, c = lane & 15;
    const __amdgpu_buffer_rsrc_t ws_r = __builtin_amdgcn_make_buffer_rsrc(a.ws, 0, 0x7fffffff, 0x00020000);
    gu32* flags = (gu32*)(a.ws + WS_FLAG);
    if (w == 0) {
        const int lo = (64 * rg64 - 256) < 0 ? 0 : (64 * rg64 - 256) / 128, hi = (64 * rg64 + 63) / 128;
        for (int r = lo; r <= hi; ++r) wait_count(flags + FL_A1 + r * 32, 3u, flags + FL_TMO);
    }
    wg_barrier();
    const float sc2 = 0.25f * 1.4426950408889634f;
    for (int it = 0; it < 8; ++it) {
        const int hd = 8 * ho + 2 * w + (it >> 2), qt = 4 * rg64 + (it & 3);
        const unsigned qb = (unsigned)(WS_AQ + (size_t)(hd * 128) * 512) + (unsigned)lane * 8;
        const unsigned kb = qb + 32u * 128u * 512u, vb = kb + 32u * 128u * 512u;
        const u32x2 qf = __builtin_amdgcn_raw_buffer_load_b64(ws_r, qb + (unsigned)qt * 512, 0, 16);
        u32x2 kf[17], vf[17];
#pragma unroll
        for (int i = 0; i < 17; ++i) {
            const int kt = qt - 16 + i, ktc = kt < 0 ? 0 : kt;
            kf[i] = __builtin_amdgcn_raw_buffer_load_b64(ws_r, kb + (unsigned)ktc * 512, 0, 16);
            vf[i] = __builtin_amdgcn_raw_buffer_load_b64(ws_r, vb + (unsigned)ktc * 512, 0, 16);
        }
        f32x4 S[17];
#pragma unroll
        for (int i = 0; i < 17; ++i) {
            f32x4 z = {0, 0, 0, 0};
            S[i] = __builtin_amdgcn_mfma_f32_16x16x16bf16_1k(__builtin_bit_cast(bf16x4, kf[i]), __builtin_bit_cast(bf16x4, qf), z, 0, 0, 0);
        }
        const float NEG = -3.0e38f;
        float m = NEG;
#pragma unroll
        for (int i = 0; i < 17; ++i) {
            const int kt = qt - 16 + i;
#pragma unroll
            for (int r = 0; r < 4; ++r) {
                bool ok = kt >= 0;
                if (i == 0) ok = ok && (4 * g + r) > c;
                if (i == 16) ok = (4 * g + r) <= c;
                S[i][r] = ok ? S[i][r] : NEG;
                m = fmaxf(m, S[i][r]);
            }
        }
        m = fmaxf(m, xor16f(m)); m = fmaxf(m, xor32f(m));
        const float nm = -m * sc2;
        float l = 0;
        u32x2 P[18];
#pragma unroll
        for (int i = 0; i < 17; ++i) {
            float p[4];
#pragma unroll
            for (int r = 0; r < 4; ++r) { p[r] = S[i][r] == NEG ? 0.f : __builtin_amdgcn_exp2f(__builtin_fmaf(S[i][r], sc2, nm)); l += p[r]; }
            P[i] = (u32x2){pk2(p[0], p[1]), pk2(p[2], p[3])};
        }
        P[17] = (u32x2){0u, 0u};
        l += xor16f(l); l += xor32f(l);
        f32x4 o = {0, 0, 0, 0};
#pragma unroll
        for (int i = 0; i < 9; ++i) {
            const u32x2 v1 = (2 * i + 1 < 17) ? vf[2 * i + 1 < 17 ? 2 * i + 1 : 0] : (u32x2){0u, 0u};
            const u32x4 va = {vf[2 * i].x, vf[2 * i].y, v1.x, v1.y};
            const u32x4 pbv = {P[2 * i].x, P[2 * i].y, P[2 * i + 1].x, P[2 * i + 1].y};
            o = __builtin_amdgcn_mfma_f32_16x16x32_bf16(__builtin_bit_cast(bf16x8, va), __builtin_bit_cast(bf16x8, pbv), o, 0, 0, 0);
        }
        const float rl = __builtin_amdgcn_rcpf(l);
        const u32x2 ov = {pk2(o[0] * rl, o[1] * rl), pk2(o[2] * rl, o[3] * rl)};
        __builtin_amdgcn_raw_buffer_store_b64(ov, ws_r, (unsigned)(WS_AO + (size_t)(qt * 16 + (hd >> 1)) * 1024) + (unsigned)(lane * 16 + (hd & 1) * 8), 0, 16);
    }
    asm volatile("s_waitcnt vmcnt(0)" ::: "memory");
    wg_barrier();
    if (tid == 0) __hip_atomic_fetch_add(flags + FL_A2 + rg64 * 32, 1u, RLX_AGENT);
}

__device__ __forceinline__ void a3_unit(const ChainArgs& a, const int un) {
    const int js = un & 1, rgp = un >> 1;
    const int tid = threadIdx.x, lane = tid & 63, w = __builtin_amdgcn_readfirstlane(tid >> 6);
    const int g = lane >> 4, c = lane & 15;
    const __amdgpu_buffer_rsrc_t ws_r = __builtin_amdgcn_make_buffer_rsrc(a.ws, 0, 0x7fffffff, 0x00020000);
    gu32* flags = (gu32*)(a.ws + WS_FLAG);
    u32x4 F0[2][16], F1[2][16];
    load_panel<1, 1, 1>(a.awo + 256 * js, 512, F0, w, lane);
    load_panel<1, 1, 1>(a.awo + 256 * js + 128, 512, F1, w, lane);
    if (w == 0) { wait_count(flags + FL_A2 + (2 * rgp) * 32, 3u, flags + FL_TMO); wait_count(flags + FL_A2 + (2 * rgp + 1) * 32, 3u, flags + FL_TMO); }
    wg_barrier();
    for (int i = 0; i < 8; ++i) {
        const int tile = rgp * 8 + i;
        u32x4 A[16];
#pragma unroll
        for (int kk = 0; kk < 16; ++kk) A[kk] = __builtin_amdgcn_raw_buffer_load_b128(ws_r, (unsigned)(WS_AO + (size_t)(tile * 16 + kk) * 1024) + (unsigned)lane * 16, 0, 16);
        f32x4 d[2][2];
        d[0][0] = (f32x4){0, 0, 0, 0}; d[0][1] = d[0][0]; d[1][0] = d[0][0]; d[1][1] = d[0][0];
        asm volatile("s_nop 1" : "+v"(d[0][0]), "+v"(d[0][1]), "+v"(d[1][0]), "+v"(d[1][1]));
#pragma unroll
        for (int kk = 0; kk < 16; ++kk)
#pragma unroll
            for (int t = 0; t < 2; ++t) { MFD(d[0][t], F0[t][kk], A[kk], "a", "v"); MFD(d[1][t], F1[t][kk], A[kk], "a", "v"); }
        asm volatile("s_nop 7" : "+v"(d[0][0]), "+v"(d[0][1]), "+v"(d[1][0]), "+v"(d[1][1]));
#pragma unroll
        for (int p = 0; p < 2; ++p)
#pragma unroll
            for (int t = 0; t < 2; ++t) {
                const u32x2 v = {pk2(d[p][t][0], d[p][t][1]), pk2(d[p][t][2], d[p][t][3])};
                *(u32x2*)(a.out + (size_t)(tile * 16 + c) * 1024 + 256 * js + 128 * p + 16 * (2 * w + t) + 4 * g) = v;
            }
    }
}

__device__ __forceinline__ void a_role(const ChainArgs& a, const int ua) {
    if (ua < 64) {
        const int j = ua & 3, rg = ua >> 2;
        if (j < 2) a1_unit<0>(a, j, rg); else if (j == 2) a1_unit<1>(a, j, rg); else a1_unit<3>(a, j, rg);
    }
    wg_barrier();
    for (int un = ua; un < 128; un += 80) a2_unit(a, un);
    wg_barrier();
    if (ua >= 48) a3_unit(a, ua - 48);
}

__global__ void __launch_bounds__(256, 1) fused_kernel(ChainArgs a) {
    extern __shared__ unsigned char smem[];
    const int b = blockIdx.x;
    const int x = b & 7, slot = b >> 3;
    if (slot < 8) { if (a.pad != 1) chain_role(a, (x + 8 * (slot >> 1)) * 2 + (slot & 1)); }
    else if (a.pad == 2) return;
    else if (slot < 16) head_role(a, (x + 8 * ((slot - 8) >> 1)) * 2 + (slot & 1));
    else if (x < NREP) { if (a.pad == 0) oproj_role(a, x * 16 + (slot - 16)); }
    else a_role(a, (x - NREP) * 16 + (slot - 16));
}

#define STOP 1000
#define DA 8
#define DB 8
extern "C" void kernel_launch(void* const* d_in, const int* in_sizes, int n_in, void* d_out, int out_size, void* d_ws, size_t ws_size, hipStream_t stream) {
    const bf16* Ax = (const bf16*)d_in[0]; const bf16* Awqkv = (const bf16*)d_in[1]; const bf16* Awo = (const bf16*)d_in[2];
    const bf16* Bx = (const bf16*)d_in[3]; const bf16* mem0 = (const bf16*)d_in[4];
    const bf16* Wq = (const bf16*)d_in[5]; const bf16* Wk = (const bf16*)d_in[6]; const bf16* Wv = (const bf16*)d_in[7]; const bf16* Wo = (const bf16*)d_in[8];
    bf16* out = (bf16*)d_out;
    unsigned char* wsb = (unsigned char*)d_ws;
    bf16* ws = (bf16*)(wsb + WS_END1);
    bf16* qkv = ws; ws += (size_t)2048 * 1536;
    bf16* oa = ws; ws += (size_t)2048 * 512;
    (void)hipMemsetAsync(wsb, 0xFF, WS_INIT_BYTES, stream);
    (void)hipFuncSetAttribute((const void*)fused_kernel, hipFuncAttributeMaxDynamicSharedMemorySize, LDS_BYTES);
    ChainArgs ca{Bx, out, Ax, Awqkv, Awo, mem0, Wq, Wk, Wv, Wo, wsb, DA, DB, STOP, 0};
    fused_kernel<<<256, 256, LDS_BYTES, stream>>>(ca);
}
```

```cpp
#include <hip/hip_runtime.h>
#include <hip/hip_bf16.h>
#include <stdint.h>
#include <type_traits>

typedef __hip_bfloat16 bf16;
typedef __bf16 bf16x8 __attribute__((ext_vector_type(8)));
typedef __bf16 bf16x4 __attribute__((ext_vector_type(4)));
typedef __bf16 bf16x2 __attribute__((ext_vector_type(2)));
typedef __bf16 bf16x16 __attribute__((ext_vector_type(16)));
typedef float f32x4 __attribute__((ext_vector_type(4)));
typedef float f32x2 __attribute__((ext_vector_type(2)));
typedef unsigned u32x4 __attribute__((ext_vector_type(4)));
typedef unsigned u32x2 __attribute__((ext_vector_type(2)));
typedef unsigned u32x8 __attribute__((ext_vector_type(8)));
#define LAS __attribute__((address_space(3)))
#define GAS __attribute__((address_space(1)))
typedef GAS unsigned gu32;
#define RLX_AGENT __ATOMIC_RELAXED, __HIP_MEMORY_SCOPE_AGENT

__device__ __forceinline__ float b2f(bf16 x) { return __bfloat162float(x); }
__device__ __forceinline__ bf16 f2b(float x) { return __float2bfloat16(x); }

#define PSLEEP 2
#define RINGN 2
constexpr int RING = RINGN;
constexpr int NVREG = 6;
#define KVRN 8
constexpr int KVR = KVRN;
#define MRN 64
constexpr int MR = MRN;
constexpr int FS = 32;
constexpr int FL_KV = 0;
constexpr int FL_KM = FL_KV + KVR * 32 * FS;
constexpr int FL_VM = FL_KM + KVR * 32 * FS;
constexpr int FL_O = FL_VM + KVR * 32 * FS;
constexpr int FL_A1 = FL_O + 64 * FS;
constexpr int FL_A2 = FL_A1 + 16 * FS;
constexpr int FL_AN = FL_A2 + 32 * FS;
constexpr int FL_RDY = FL_AN + 64 * FS;
constexpr int FL_TMO = FL_RDY + FS;
constexpr int FL_END = FL_TMO + FS;
constexpr size_t WS_FLAG = 0;
constexpr size_t WS_MEMS = WS_FLAG + (size_t)FL_END * 4;
constexpr size_t WS_INIT_BYTES = WS_MEMS + (size_t)MR * 8192;
constexpr size_t WS_PART = (WS_INIT_BYTES + 4095) / 4096 * 4096;
constexpr size_t WS_KX = WS_PART + (size_t)RING * 64 * 8192;
constexpr size_t WS_VX = WS_KX + (size_t)KVR * 32 * 8192;
constexpr size_t WS_KM = WS_VX + (size_t)KVR * 32 * 8192;
constexpr size_t WS_VM = WS_KM + (size_t)KVR * 32 * 2048;
constexpr size_t WS_OX = WS_VM + (size_t)KVR * 32 * 2048;
constexpr size_t WS_AQ = WS_OX + (size_t)64 * 2 * 128 * 1024;
constexpr size_t WS_AO = WS_AQ + (size_t)3 * 32 * 128 * 512;
constexpr size_t WS_END1 = WS_AO + (size_t)128 * 16 * 1024;
static_assert(WS_INIT_BYTES % 16 == 0, "memset size");

constexpr unsigned SPIN_CAP = 1u << 20;

constexpr unsigned L_MEM = 0;
constexpr unsigned L_KX = 8192;
constexpr unsigned L_VX = L_KX + 2 * 12288;
constexpr unsigned L_Q = L_VX + 2 * 8192;
constexpr unsigned L_O = L_Q + 4096;
constexpr unsigned L_VM = L_O + 2048;
constexpr unsigned L_FLAG = L_VM + 2048;
constexpr unsigned L_DUMMY = L_FLAG + 64;
constexpr unsigned L_WV = 65536;
constexpr unsigned L_WO = 98304;
constexpr unsigned L_STG = 0;
constexpr unsigned LDS_BYTES = 160 * 1024;

__device__ __forceinline__ u32x4 lds_rd16(unsigned a) { return *(LAS u32x4*)(size_t)a; }
__device__ __forceinline__ void lds_wr16(unsigned a, u32x4 v) { *(LAS u32x4*)(size_t)a = v; }
__device__ __forceinline__ void lds_wr4(unsigned a, unsigned v) { *(LAS unsigned*)(size_t)a = v; }
__device__ __forceinline__ void lds_wr8(unsigned a, u32x2 v) { *(LAS u32x2*)(size_t)a = v; }
__device__ __forceinline__ unsigned lds_rd4(unsigned a) { return *(LAS unsigned*)(size_t)a; }
__device__ __forceinline__ u32x2 lds_rd8(unsigned a) { return *(LAS u32x2*)(size_t)a; }
__device__ __forceinline__ void wg_barrier() { asm volatile("s_waitcnt lgkmcnt(0)" ::: "memory"); __builtin_amdgcn_s_barrier(); asm volatile("" ::: "memory"); }
__device__ __forceinline__ unsigned pk2(float a, float b) { bf16x2 v = {(__bf16)a, (__bf16)b}; return __builtin_bit_cast(unsigned, v); }
__device__ __forceinline__ unsigned max4(u32x4 a) { return max(max(a.x, a.y), max(a.z, a.w)); }
__device__ __forceinline__ void dma16(const __amdgpu_buffer_rsrc_t r, unsigned voff, unsigned lds_dst) {
    unsigned keep;
    asm volatile("s_mov_b32 %0, m0\n\ts_mov_b32 m0, %3\n\ts_nop 0\n\tbuffer_load_dwordx4 %1, %2, 0 offen sc1 lds\n\ts_mov_b32 m0, %0" : "=&s"(keep) : "v"(voff), "s"(r), "s"(lds_dst) : "memory");
}
__device__ __forceinline__ float xor8f(float v) { return __builtin_bit_cast(float, __builtin_amdgcn_update_dpp(0, __builtin_bit_cast(int, v), 0x128  , 0xf, 0xf, false)); }
__device__ __forceinline__ float xor16f(float v) {
    auto r = __builtin_amdgcn_permlane16_swap(__builtin_bit_cast(unsigned, v), __builtin_bit_cast(unsigned, v), false, false);
    return __builtin_bit_cast(float, (threadIdx.x & 16) ? r[0] : r[1]);
}
__device__ __forceinline__ float xor32f(float v) {
    auto r = __builtin_amdgcn_permlane32_swap(__builtin_bit_cast(unsigned, v), __builtin_bit_cast(unsigned, v), false, false);
    return __builtin_bit_cast(float, (threadIdx.x & 32) ? r[0] : r[1]);
}

__device__ __forceinline__ void tr8x8(const u32x4 (&r)[8], u32x4 (&f)[8]) {
#pragma unroll
    for (int t = 0; t < 8; ++t) {
        const unsigned sel = (t & 1) ? 0x07060302u : 0x05040100u;
#pragma unroll
        for (int d = 0; d < 4; ++d) f[t][d] = __builtin_amdgcn_perm(r[2 * d + 1][t >> 1], r[2 * d][t >> 1], sel);
    }
}

#define SMF_A(acc, A_, W_) asm volatile("v_smfmac_f32_16x16x64_bf16 %0, %1, %2, %3" : "+v"(acc) : "v"(A_), "a"(W_), "v"(idx))
#define SMF_V(acc, A_, W_) asm volatile("v_smfmac_f32_16x16x64_bf16 %0, %1, %2, %3" : "+v"(acc) : "v"(A_), "v"(W_), "v"(idx))
#define SMF_VN(acc, A_, W_) asm volatile("s_nop 1\n\tv_smfmac_f32_16x16x64_bf16 %0, %1, %2, %3" : "+v"(acc) : "v"(A_), "v"(W_), "v"(idx))
__device__ __forceinline__ float vadd(float a, float b) { float r; asm("v_add_f32 %0, %1, %2" : "=v"(r) : "v"(a), "v"(b)); return r; }
__device__ __forceinline__ float vmax3(float a, float b, float c) { float r; asm("v_max3_f32 %0, %1, %2, %3" : "=v"(r) : "v"(a), "v"(b), "v"(c)); return r; }
__device__ __forceinline__ float vmax2(float a, float b) { float r; asm("v_max_f32 %0, %1, %2" : "=v"(r) : "v"(a), "v"(b)); return r; }
__device__ __forceinline__ unsigned vnosent(float a) { unsigned r; asm("v_min_u32 %0, %1, %2" : "=v"(r) : "v"(a), "v"(0xFFFFFFFEu)); return r; }
__device__ __forceinline__ void swap16(float& a, float& b) { asm volatile("s_nop 1\n\tv_permlane16_swap_b32 %0, %1" : "+v"(a), "+v"(b)); }
__device__ __forceinline__ void swap32(float& a, float& b) { asm volatile("s_nop 1\n\tv_permlane32_swap_b32 %0, %1" : "+v"(a), "+v"(b)); }
__device__ __forceinline__ void pack_rows(const f32x4 a, const f32x4 b, unsigned& r0, unsigned& r1) {
    float t0, t1, t2, t3;
    asm("v_add_f32 %2, %6, %7\n\tv_add_f32 %3, %10, %11\n\tv_add_f32 %4, %8, %9\n\tv_add_f32 %5, %12, %13\n\t"
        "v_cvt_pk_bf16_f32 %0, %2, %3\n\tv_cvt_pk_bf16_f32 %1, %4, %5"
        : "=&v"(r0), "=&v"(r1), "=&v"(t0), "=&v"(t1), "=&v"(t2), "=&v"(t3)
        : "v"(a[0]), "v"(a[1]), "v"(a[2]), "v"(a[3]), "v"(b[0]), "v"(b[1]), "v"(b[2]), "v"(b[3]));
}
__device__ __forceinline__ void pack_cols(const f32x4 a, const f32x4 b, unsigned& r0, unsigned& r1) {
    float t0, t1, t2, t3;
    asm("v_add_f32 %2, %6, %7\n\tv_add_f32 %3, %8, %9\n\tv_add_f32 %4, %10, %11\n\tv_add_f32 %5, %12, %13\n\t"
        "v_cvt_pk_bf16_f32 %0, %2, %3\n\tv_cvt_pk_bf16_f32 %1, %4, %5"
        : "=&v"(r0), "=&v"(r1), "=&v"(t0), "=&v"(t1), "=&v"(t2), "=&v"(t3)
        : "v"(a[0]), "v"(a[1]), "v"(a[2]), "v"(a[3]), "v"(b[0]), "v"(b[1]), "v"(b[2]), "v"(b[3]));
}
__device__ __forceinline__ float neg_max_scaled(const f32x4 S0, const f32x4 S1, const f32x4 S2, float sc) {
    float m, t;
    asm("v_max3_f32 %0, %2, %3, %4\n\tv_max3_f32 %1, %5, %6, %7\n\tv_max3_f32 %0, %0, %8, %9\n\tv_max3_f32 %1, %1, %10, %11\n\tv_max_f32 %0, %0, %1\n\t"
        "v_mov_b32 %1, %0\n\ts_nop 1\n\tv_permlane16_swap_b32 %0, %1\n\tv_max_f32 %0, %0, %1\n\t"
        "v_mov_b32 %1, %0\n\ts_nop 1\n\tv_permlane32_swap_b32 %0, %1\n\tv_max_f32 %0, %0, %1\n\tv_mul_f32 %0, %0, %12"
        : "=&v"(m), "=&v"(t)
        : "v"(S1[0]), "v"(S1[1]), "v"(S1[2]), "v"(S1[3]), "v"(S2[0]), "v"(S2[1]), "v"(S0[0]), "v"(S0[1]), "v"(S0[2]), "v"(S0[3]), "v"(sc));
    return m;
}
__device__ __forceinline__ float recip_sum(const float (&p0)[4], const float (&p1)[4], const float (&p2)[2]) {
    float a, b, c;
    asm("v_add_f32 %0, %3, %4\n\tv_add_f32 %1, %5, %6\n\tv_add_f32 %2, %7, %8\n\tv_add_f32 %0, %0, %9\n\tv_add_f32 %1, %1, %10\n\tv_add_f32 %2, %2, %11\n\t"
        "v_add_f32 %0, %0, %12\n\tv_add_f32 %1, %1, %2\n\tv_add_f32 %0, %0, %1\n\t"
        "v_mov_b32 %1, %0\n\ts_nop 1\n\tv_permlane16_swap_b32 %0, %1\n\tv_add_f32 %0, %0, %1\n\t"
        "v_mov_b32 %1, %0\n\ts_nop 1\n\tv_permlane32_swap_b32 %0, %1\n\tv_add_f32 %0, %0, %1\n\tv_rcp_f32 %0, %0\n\ts_nop 0"
        : "=&v"(a), "=&v"(b), "=&v"(c)
        : "v"(p0[0]), "v"(p0[1]), "v"(p0[2]), "v"(p0[3]), "v"(p1[0]), "v"(p1[1]), "v"(p1[2]), "v"(p1[3]), "v"(p2[0]), "v"(p2[1]));
    return a;
}
__device__ __forceinline__ u32x4 scale_pack(const f32x4 a, const f32x4 b, float r) {
    u32x4 o; float t0, t1;
    asm("v_mul_f32 %4, %6, %14\n\tv_mul_f32 %5, %10, %14\n\tv_cvt_pk_bf16_f32 %0, %4, %5\n\t"
        "v_mul_f32 %4, %7, %14\n\tv_mul_f32 %5, %11, %14\n\tv_cvt_pk_bf16_f32 %1, %4, %5\n\t"
        "v_mul_f32 %4, %8, %14\n\tv_mul_f32 %5, %12, %14\n\tv_cvt_pk_bf16_f32 %2, %4, %5\n\t"
        "v_mul_f32 %4, %9, %14\n\tv_mul_f32 %5, %13, %14\n\tv_cvt_pk_bf16_f32 %3, %4, %5"
        : "=&v"(o.x), "=&v"(o.y), "=&v"(o.z), "=&v"(o.w), "=&v"(t0), "=&v"(t1)
        : "v"(a[0]), "v"(a[1]), "v"(a[2]), "v"(a[3]), "v"(b[0]), "v"(b[1]), "v"(b[2]), "v"(b[3]), "v"(r));
    return o;
}
__device__ __forceinline__ u32x4 part_pack(const f32x4 a, const f32x4 b, unsigned lim) {
    u32x4 o;
    asm("v_add_f32 %0, %4, %5\n\tv_add_f32 %1, %8, %9\n\tv_add_f32 %2, %6, %7\n\tv_add_f32 %3, %10, %11\n\t"
        "v_min_u32 %0, %0, %12\n\tv_min_u32 %1, %1, %12\n\tv_min_u32 %2, %2, %12\n\tv_min_u32 %3, %3, %12"
        : "=&v"(o.x), "=&v"(o.y), "=&v"(o.z), "=&v"(o.w)
        : "v"(a[0]), "v"(a[1]), "v"(a[2]), "v"(a[3]), "v"(b[0]), "v"(b[1]), "v"(b[2]), "v"(b[3]), "s"(lim));
    return o;
}
__device__ __forceinline__ float max8_x(const f32x4 a, const f32x4 b) {
    float m, t;
    asm("v_max3_f32 %0, %2, %3, %4\n\tv_max3_f32 %1, %5, %6, %7\n\tv_max3_f32 %0, %0, %8, %9\n\tv_max_f32 %0, %0, %1\n\t"
        "v_mov_b32 %1, %0\n\ts_nop 1\n\tv_permlane16_swap_b32 %0, %1\n\tv_max_f32 %0, %0, %1\n\t"
        "v_mov_b32 %1, %0\n\ts_nop 1\n\tv_permlane32_swap_b32 %0, %1\n\tv_max_f32 %0, %0, %1"
        : "=&v"(m), "=&v"(t)
        : "v"(a[0]), "v"(a[1]), "v"(a[2]), "v"(a[3]), "v"(b[0]), "v"(b[1]), "v"(b[2]), "v"(b[3]));
    return m;
}
__device__ __forceinline__ float max2_x(float a, float b, float mx) {
    float m, t;
    asm("v_max_f32 %0, %2, %3\n\t"
        "v_mov_b32 %1, %0\n\ts_nop 1\n\tv_permlane16_swap_b32 %0, %1\n\tv_max_f32 %0, %0, %1\n\t"
        "v_mov_b32 %1, %0\n\ts_nop 1\n\tv_permlane32_swap_b32 %0, %1\n\tv_max3_f32 %0, %0, %1, %4"
        : "=&v"(m), "=&v"(t) : "v"(a), "v"(b), "v"(mx));
    return m;
}
__device__ __forceinline__ float sum8_x(const float (&p)[4], const float (&q)[4]) {
    float a, b;
    asm("v_add_f32 %0, %2, %3\n\tv_add_f32 %1, %4, %5\n\tv_add_f32 %0, %0, %6\n\tv_add_f32 %1, %1, %7\n\tv_add_f32 %0, %0, %8\n\tv_add_f32 %1, %1, %9\n\tv_add_f32 %0, %0, %1\n\t"
        "v_mov_b32 %1, %0\n\ts_nop 1\n\tv_permlane16_swap_b32 %0, %1\n\tv_add_f32 %0, %0, %1\n\t"
        "v_mov_b32 %1, %0\n\ts_nop 1\n\tv_permlane32_swap_b32 %0, %1\n\tv_add_f32 %0, %0, %1"
        : "=&v"(a), "=&v"(b)
        : "v"(p[0]), "v"(p[1]), "v"(p[2]), "v"(p[3]), "v"(q[0]), "v"(q[1]), "v"(q[2]), "v"(q[3]));
    return a;
}
__device__ __forceinline__ float recip_l(float a, float b, float al, float lx) {
    float r, t;
    asm("v_add_f32 %0, %2, %3\n\t"
        "v_mov_b32 %1, %0\n\ts_nop 1\n\tv_permlane16_swap_b32 %0, %1\n\tv_add_f32 %0, %0, %1\n\t"
        "v_mov_b32 %1, %0\n\ts_nop 1\n\tv_permlane32_swap_b32 %0, %1\n\tv_add_f32 %0, %0, %1\n\tv_fma_f32 %0, %4, %5, %0\n\tv_rcp_f32 %0, %0\n\ts_nop 0"
        : "=&v"(r), "=&v"(t) : "v"(a), "v"(b), "v"(al), "v"(lx));
    return r;
}
__device__ __forceinline__ u32x4 pack_o(const f32x4 a, const f32x4 b) {
    u32x4 o;
    asm("v_cvt_pk_bf16_f32 %0, %4, %8\n\tv_cvt_pk_bf16_f32 %1, %5, %9\n\tv_cvt_pk_bf16_f32 %2, %6, %10\n\tv_cvt_pk_bf16_f32 %3, %7, %11"
        : "=&v"(o.x), "=&v"(o.y), "=&v"(o.z), "=&v"(o.w)
        : "v"(a[0]), "v"(a[1]), "v"(a[2]), "v"(a[3]), "v"(b[0]), "v"(b[1]), "v"(b[2]), "v"(b[3]));
    return o;
}
__device__ __forceinline__ float neg_max10(const f32x4 m_, const f32x4 a, const f32x4 b, float sc) {
    float m, t;
    asm("v_max3_f32 %0, %2, %3, %4\n\tv_max3_f32 %1, %5, %6, %7\n\tv_max3_f32 %0, %0, %8, %9\n\tv_max3_f32 %1, %1, %10, %11\n\tv_max_f32 %0, %0, %1\n\t"
        "v_mov_b32 %1, %0\n\ts_nop 1\n\tv_permlane16_swap_b32 %0, %1\n\tv_max_f32 %0, %0, %1\n\t"
        "v_mov_b32 %1, %0\n\ts_nop 1\n\tv_permlane32_swap_b32 %0, %1\n\tv_max_f32 %0, %0, %1\n\tv_mul_f32 %0, %0, %12"
        : "=&v"(m), "=&v"(t)
        : "v"(a[0]), "v"(a[1]), "v"(a[2]), "v"(a[3]), "v"(b[0]), "v"(b[1]), "v"(b[2]), "v"(b[3]), "v"(m_[0]), "v"(m_[1]), "v"(sc));
    return m;
}
__device__ __forceinline__ float recip_sum10(const float (&p0)[4], const float (&p1)[4], float q0, float q1) {
    float a, b, c;
    asm("v_add_f32 %0, %3, %4\n\tv_add_f32 %1, %5, %6\n\tv_add_f32 %2, %7, %8\n\tv_add_f32 %0, %0, %9\n\tv_add_f32 %1, %1, %10\n\tv_add_f32 %2, %2, %11\n\t"
        "v_add_f32 %0, %0, %12\n\tv_add_f32 %1, %1, %2\n\tv_add_f32 %0, %0, %1\n\t"
        "v_mov_b32 %1, %0\n\ts_nop 1\n\tv_permlane16_swap_b32 %0, %1\n\tv_add_f32 %0, %0, %1\n\t"
        "v_mov_b32 %1, %0\n\ts_nop 1\n\tv_permlane32_swap_b32 %0, %1\n\tv_add_f32 %0, %0, %1\n\tv_rcp_f32 %0, %0\n\ts_nop 0"
        : "=&v"(a), "=&v"(b), "=&v"(c)
        : "v"(p0[0]), "v"(p0[1]), "v"(p0[2]), "v"(p0[3]), "v"(p1[0]), "v"(p1[1]), "v"(p1[2]), "v"(p1[3]), "v"(q0), "v"(q1));
    return a;
}
#define MF0(acc, A_, B_) asm volatile("v_mfma_f32_16x16x32_bf16 %0, %1, %2, 0" : "=&v"(acc) : "v"(A_), "v"(B_))
#define MFA(acc, A_, B_) asm volatile("v_mfma_f32_16x16x32_bf16 %0, %1, %2, %0" : "+v"(acc) : "v"(A_), "v"(B_))
struct ChainArgs {
    const bf16* x; bf16* out; const bf16* ax; const bf16* awqkv; const bf16* awo;
    const bf16* mem0; const bf16* Wq; const bf16* Wk; const bf16* Wv; const bf16* Wo;
    unsigned char* ws;
    int dA, dB, gA, gB, stop, pad, cS, pad2;
};

__device__ __forceinline__ bool wait_flag(gu32* f, unsigned want, gu32* tmo) {
    for (unsigned spins = 0;; ++spins) {
        const unsigned v = __builtin_amdgcn_readfirstlane(__hip_atomic_load(f, RLX_AGENT));
        if (v == want) return true;
        if (spins > SPIN_CAP) { __hip_atomic_store(tmo, 1u, RLX_AGENT); return false; }
        __builtin_amdgcn_s_sleep(PSLEEP);
    }
}

__device__ __forceinline__ void chain_role(const ChainArgs& a, const int b) {
    const int h = b >> 1, nh = b & 1;
    const int tid = threadIdx.x, lane = tid & 63, w = __builtin_amdgcn_readfirstlane(tid >> 6);
    const int g = lane >> 4, c = lane & 15;
    const __amdgpu_buffer_rsrc_t ws_r = __builtin_amdgcn_make_buffer_rsrc(a.ws, 0, 0x7fffffff, 0x00020000);
    gu32* flags = (gu32*)(a.ws + WS_FLAG);
    gu32* tmo = flags + FL_TMO;

    {
        const u32x4 pz = {~0u, ~0u, ~0u, ~0u};
#pragma unroll
        for (int sl = 0; sl < RING; ++sl)
#pragma unroll
            for (int k = 0; k < 2; ++k) __builtin_amdgcn_raw_buffer_store_b128(pz, ws_r, (unsigned)((k * 256 + tid) * 16), (unsigned)(WS_PART + (size_t)(sl * 64 + b) * 8192), 16);
        asm volatile("s_waitcnt vmcnt(0)" ::: "memory");
        wg_barrier();
        if (tid == 0) __hip_atomic_fetch_add(flags + FL_RDY, 1u, RLX_AGENT);
    }
    u32x8 W[3][2][8];
    u32x4 pend[2 * (8 - NVREG) * 2];
    {
        const int rj[8] = {0, 1, 8, 9, 2, 3, 10, 11};
        const int rj2[8] = {0, 1, 2, 3, 8, 9, 10, 11};
        u32x4 raw[4][8];
        auto ld_raw = [&](const bf16* Wm) {
#pragma unroll
            for (int it = 0; it < 4; ++it) {
                const int rb = 32 * (4 * w + it) + 16 * (g >> 1) + 4 * (g & 1);
#pragma unroll
                for (int j = 0; j < 8; ++j) raw[it][j] = *(const u32x4*)(Wm + (size_t)(rb + rj[j]) * 4096);
            }
        };
        ld_raw(a.Wk + h * 128 + 8 * c);
#pragma unroll
        for (int Mi = 0; Mi < 3; ++Mi) {
            const int M = Mi == 0 ? 1 : (Mi == 1 ? 0 : 2);
#pragma unroll
            for (int it = 0; it < 4; ++it) {
                u32x4 f[8];
                tr8x8(raw[it], f);
#pragma unroll
                for (int t = 0; t < 8; ++t) lds_wr16(L_STG + (unsigned)(((t * 16 + 4 * w + it) * 64 + lane) * 16), f[t]);
            }
            if (Mi == 0) ld_raw(a.Wq + h * 128 + 8 * c);
            if (Mi == 1) ld_raw(a.Wv + h * 128 + 8 * c);
            if (Mi == 2) {
#pragma unroll
                for (int half = 0; half < 2; ++half) {
                    const int rb = 32 * w + 16 * (g & 1) + 4 * (g >> 1);
                    const bf16* Wp = a.Wo + (size_t)h * 128 * 512 + 256 * nh + 8 * (16 * half + c);
#pragma unroll
                    for (int j = 0; j < 8; ++j) raw[half][j] = *(const u32x4*)(Wp + (size_t)(rb + rj2[j]) * 512);
                }
            }
            wg_barrier();
#pragma unroll
            for (int tau = 0; tau < 2; ++tau)
#pragma unroll
                for (int kk = 0; kk < 8; ++kk) {
                    const int kr = (kk + 2 * w) & 7;
                    const u32x4 lo = lds_rd16(L_STG + (unsigned)((((2 * w + tau) * 16 + 2 * kr) * 64 + lane) * 16));
                    const u32x4 hi = lds_rd16(L_STG + (unsigned)((((2 * w + tau) * 16 + 2 * kr + 1) * 64 + lane) * 16));
                    u32x8 v = {lo.x, lo.y, lo.z, lo.w, hi.x, hi.y, hi.z, hi.w};
                    W[M][tau][kk] = v;
                    if (M < 2) asm volatile("" : "+a"(W[M][tau][kk]));
                    else if (kk >= NVREG) {
                        pend[(tau * (8 - NVREG) + kk - NVREG) * 2] = lo; pend[(tau * (8 - NVREG) + kk - NVREG) * 2 + 1] = hi;
                    }
                }
            wg_barrier();
        }
#pragma unroll
        for (int q2 = 0; q2 < 2 * (8 - NVREG) * 2; ++q2) lds_wr16(L_WV + (unsigned)(((w * 2 * (8 - NVREG) * 2 + q2) * 64 + lane) * 16), pend[q2]);
#pragma unroll
        for (int half = 0; half < 2; ++half) {
            const int m = 16 * half + c;
            u32x4 f[8];
            tr8x8(raw[half], f);
#pragma unroll
            for (int t = 0; t < 8; ++t) {
                const int n = 8 * m + t;
                const int ww = n >> 6, n6 = n & 63, G4 = n6 >> 4, n4 = n6 & 15;
                const int tp = 2 * (G4 >> 1) + (n4 & 1), cc = 8 * (G4 & 1) + (n4 >> 1);
                lds_wr16(L_WO + (unsigned)((((ww * 4 + tp) * 4 + w) * 64 + g * 16 + cc) * 16), f[t]);
            }
        }
    }
    u32x4 m0, m1;
    m0 = *(const u32x4*)(a.mem0 + (c >> 1) * 512 + 64 * (2 * w) + 16 * g + 8 * (c & 1));
    m1 = *(const u32x4*)(a.mem0 + (c >> 1) * 512 + 64 * (2 * w + 1) + 16 * g + 8 * (c & 1));
    lds_wr16(L_MEM + (unsigned)((2 * w * 64 + lane) * 16), m0);
    lds_wr16(L_MEM + (unsigned)(((2 * w + 1) * 64 + lane) * 16), m1);
    if (w == 1 || w == 2) {
        wait_flag(flags + FL_KV + (0 * 32 + h) * FS, 1u, tmo);
        const unsigned src = (unsigned)((w == 1 ? WS_KX : WS_VX) + (size_t)(0 * 32 + h) * 8192);
        const unsigned dst = (w == 1 ? L_KX : L_VX);
#pragma unroll
        for (int k = 0; k < 8; ++k) dma16(ws_r, src + k * 1024 + lane * 16, dst + k * 1024);
        asm volatile("s_waitcnt vmcnt(0)" ::: "memory");
    }
    if (w == 0) wait_flag(flags + FL_RDY, 63u, tmo);
    if (tid == 0) lds_wr4(L_FLAG, 0);
    wg_barrier();

    const int idx = (c & 1) ? 0xEEEE : 0x4444;
    const float sc2 = 0.08838834764831845f * 1.4426950408889634f;
    unsigned epos = (unsigned)(((c >> 2) * 64 + (2 * (c & 1) + (w >> 1)) * 16 + 2 * g) * 16 + (4 * ((c >> 1) & 1) + 2 * (w & 1)) * 2);
    const int gp = c >> 2, rp = c & 3;
    unsigned qwo = L_Q + epos;
    unsigned l16 = (unsigned)lane * 16;
    unsigned a0o = 8192u + (unsigned)((g * 16 + 2 * gp + (rp & 1)) * 16);
    unsigned a2o = 4096u + (unsigned)((g * 16 + 4 * gp + 2 + (rp & 1)) * 16);
    unsigned qao = L_Q + (unsigned)((g * 16 + (c & 7)) * 16);
    unsigned wvb = L_WV + (unsigned)(w * 2 * (8 - NVREG) * 2 * 1024) + l16;
    unsigned wob = L_WO + (unsigned)(w * 16384) + l16;
    unsigned owo = (c < 8) ? L_O + (unsigned)(((g >> 1) * 64 + (2 * (g & 1) + (w >> 1)) * 16 + 2 * (c & 7) + (w & 1)) * 16) : L_DUMMY + (unsigned)tid * 16;
    unsigned pvo = (unsigned)((((2 * (16 * nh + 4 * w + (c >> 3)) + (g >> 1)) * 32 + h) * 2 + (g & 1)) * 128 + (c & 7) * 16);

    asm volatile("" : "+v"(l16), "+v"(a0o), "+v"(a2o), "+v"(qao), "+v"(wvb), "+v"(wob), "+v"(owo), "+v"(pvo), "+v"(epos), "+v"(qwo));
    f32x4 aq[2], ak[2], av[2];
    u32x4 pm0 = {0u, 0u, 0u, 0u}, pm1 = pm0, vmt = pm0;
#define ZACC() do { _Pragma("unroll") for (int t = 0; t < 2; ++t) { aq[t] = (f32x4){0, 0, 0, 0}; ak[t] = aq[t]; av[t] = aq[t]; } \
        asm volatile("" : "+v"(aq[0]), "+v"(aq[1]), "+v"(ak[0]), "+v"(ak[1]), "+v"(av[0]), "+v"(av[1])); } while (0)
    ZACC();
#define EARLY() do { asm volatile("s_nop 1" : "+v"(m0), "+v"(m1), "+v"(ak[0]), "+v"(ak[1]), "+v"(aq[0]), "+v"(aq[1])); \
        SMF_A(ak[0], m0, W[1][0][0]); SMF_A(ak[1], m0, W[1][1][0]); SMF_A(aq[0], m0, W[0][0][0]); SMF_A(aq[1], m0, W[0][1][0]); \
        SMF_A(ak[0], m1, W[1][0][1]); SMF_A(ak[1], m1, W[1][1][1]); SMF_A(aq[0], m1, W[0][0][1]); SMF_A(aq[1], m1, W[0][1][1]); } while (0)
    EARLY();
    unsigned amo[6];
#pragma unroll
    for (int j = 0; j < 6; ++j) { amo[j] = L_MEM + l16 + (unsigned)(((j + 2 + 2 * w) & 7) * 1024); asm volatile("" : "+v"(amo[j])); }
    for (int s = 0; s < 64; ++s) {
        const unsigned kxb = L_KX + (unsigned)(s & 1) * 12288, vxb = L_VX + (unsigned)(s & 1) * 8192;
        unsigned kvflag = (unsigned)(s + 2);
        if ((w == 1 || w == 2) && s < 63) kvflag = __hip_atomic_load(flags + FL_KV + (((s + 1) % KVR) * 32 + h) * FS, RLX_AGENT);
#define SB() __builtin_amdgcn_sched_barrier(0)
        u32x4 A[8];
        const unsigned kxl = kxb + l16, k0l = kxb + a0o, vxl = vxb + (unsigned)(2 * w * 1024) + l16, kml = kxb + 8192 + epos;
        u32x4 ka[4], kb[4], vx[2], qf[4], k0[4], wl[8], wq[16];
#define RDA(k) A[k] = lds_rd16(amo[(k) - 2])
#define RDWL(i) wl[i] = lds_rd16(wvb + (unsigned)((i) * 1024))
#define RDWO(i) wq[i] = lds_rd16(wob + (unsigned)((i) * 1024))
#define CAT8(lo, hi) ((u32x8){lo.x, lo.y, lo.z, lo.w, hi.x, hi.y, hi.z, hi.w})
        A[0] = m0; A[1] = m1;
        RDA(2); RDA(3); RDA(4);
        SB();
        SMF_A(ak[0], A[2], W[1][0][2]); RDA(5); SB(); SMF_A(ak[1], A[2], W[1][1][2]); RDA(6); SB();
        SMF_A(ak[0], A[3], W[1][0][3]); RDA(7); SB(); SMF_A(ak[1], A[3], W[1][1][3]); RDWL(0); SB();
        SMF_A(ak[0], A[4], W[1][0][4]); RDWL(1); SB(); SMF_A(ak[1], A[4], W[1][1][4]); RDWL(2); SB();
        SMF_A(ak[0], A[5], W[1][0][5]); RDWL(3); SB(); SMF_A(ak[1], A[5], W[1][1][5]); RDWL(4); SB();
        SMF_A(ak[0], A[6], W[1][0][6]); RDWL(5); SB(); SMF_A(ak[1], A[6], W[1][1][6]); RDWL(6); SB();
        SMF_A(ak[0], A[7], W[1][0][7]); RDWL(7); SB(); SMF_A(ak[1], A[7], W[1][1][7]);
#pragma unroll
        for (int kk = 2; kk < 5; ++kk) { SMF_A(aq[0], A[kk], W[0][0][kk]); SMF_A(aq[1], A[kk], W[0][1][kk]); }
        asm volatile("s_nop 1" : "+v"(ak[0]), "+v"(ak[1]));
        { unsigned r0, r1; pack_rows(ak[0], ak[1], r0, r1); lds_wr4(kml, r0); lds_wr4(kml + 16, r1); }
        SB();
#pragma unroll
        for (int kk = 5; kk < 8; ++kk) { SMF_A(aq[0], A[kk], W[0][0][kk]); SMF_A(aq[1], A[kk], W[0][1][kk]); }
        { const u32x8 w0 = CAT8(wl[0], wl[1]), w1 = CAT8(wl[2], wl[3]), w2 = CAT8(wl[4], wl[5]), w3 = CAT8(wl[6], wl[7]);
          SMF_V(av[0], A[6], w0); SMF_V(av[1], A[6], w2); SMF_V(av[0], A[7], w1); SMF_V(av[1], A[7], w3); }
        asm volatile("s_nop 3" : "+v"(aq[0]), "+v"(aq[1]));
        { unsigned r0, r1; pack_rows(aq[0], aq[1], r0, r1); lds_wr4(qwo, r0); lds_wr4(qwo + 16, r1); }
        SB();
        SMF_V(av[0], A[0], W[2][0][0]); ka[0] = lds_rd16(kxl); SB(); SMF_V(av[1], A[0], W[2][1][0]); kb[0] = lds_rd16(kxl + 4096); SB();
        SMF_V(av[0], A[1], W[2][0][1]); ka[1] = lds_rd16(kxl + 1024); SB(); SMF_V(av[1], A[1], W[2][1][1]); SB();
        asm volatile("s_waitcnt lgkmcnt(3)" ::: "memory");
        __builtin_amdgcn_s_barrier(); asm volatile("" ::: "memory");
        qf[0] = lds_rd16(qao); k0[0] = lds_rd16(k0l); SB();
        SMF_V(av[0], A[2], W[2][0][2]); qf[1] = lds_rd16(qao + 1024); SB(); SMF_V(av[1], A[2], W[2][1][2]); k0[1] = lds_rd16(k0l + 1024); SB();
        SMF_V(av[0], A[3], W[2][0][3]); kb[1] = lds_rd16(kxl + 4096 + 1024); SB(); SMF_V(av[1], A[3], W[2][1][3]); qf[2] = lds_rd16(qao + 2048); SB();
        f32x4 Sa, Sb, Sm;
        MF0(Sm, k0[0], qf[0]); k0[2] = lds_rd16(k0l + 2048); SB();
        MF0(Sa, ka[0], qf[0]); ka[2] = lds_rd16(kxl + 2048); SB();
        MF0(Sb, kb[0], qf[0]); kb[2] = lds_rd16(kxl + 4096 + 2048); SB();
        MFA(Sm, k0[1], qf[1]); qf[3] = lds_rd16(qao + 3072); SB();
        MFA(Sa, ka[1], qf[1]); k0[3] = lds_rd16(k0l + 3072); SB();
        MFA(Sb, kb[1], qf[1]); ka[3] = lds_rd16(kxl + 3072); SB();
        MFA(Sm, k0[2], qf[2]); kb[3] = lds_rd16(kxl + 4096 + 3072); SB();
        MFA(Sa, ka[2], qf[2]); vx[0] = lds_rd16(vxl); SB();
        MFA(Sb, kb[2], qf[2]); vx[1] = lds_rd16(vxl + 1024); SB();
        MFA(Sm, k0[3], qf[3]); RDWO(0); SB();
        MFA(Sa, ka[3], qf[3]); RDWO(1); SB();
        MFA(Sb, kb[3], qf[3]); RDWO(2); SB();
        SMF_V(av[0], A[4], W[2][0][4]); RDWO(3); SB(); SMF_V(av[1], A[4], W[2][1][4]); RDWO(4); SB();
        SMF_V(av[0], A[5], W[2][0][5]); RDWO(5); SB(); SMF_V(av[1], A[5], W[2][1][5]); RDWO(6); SB();
        asm volatile("" : "+v"(Sm), "+v"(Sa), "+v"(Sb));
        const float nmb = neg_max10(Sm, Sa, Sb, -sc2);
        float pa[4], pb[4];
        RDWO(7);
        pa[0] = __builtin_amdgcn_exp2f(__builtin_fmaf(Sa[0], sc2, nmb)); pa[1] = __builtin_amdgcn_exp2f(__builtin_fmaf(Sa[1], sc2, nmb)); RDWO(8); SB();
        pa[2] = __builtin_amdgcn_exp2f(__builtin_fmaf(Sa[2], sc2, nmb)); pa[3] = __builtin_amdgcn_exp2f(__builtin_fmaf(Sa[3], sc2, nmb)); RDWO(9); SB();
        pb[0] = __builtin_amdgcn_exp2f(__builtin_fmaf(Sb[0], sc2, nmb)); pb[1] = __builtin_amdgcn_exp2f(__builtin_fmaf(Sb[1], sc2, nmb)); RDWO(10); SB();
        pb[2] = __builtin_amdgcn_exp2f(__builtin_fmaf(Sb[2], sc2, nmb)); pb[3] = __builtin_amdgcn_exp2f(__builtin_fmaf(Sb[3], sc2, nmb)); RDWO(11); SB();
        const float pm_0 = __builtin_amdgcn_exp2f(__builtin_fmaf(Sm[0], sc2, nmb)), pm_1 = __builtin_amdgcn_exp2f(__builtin_fmaf(Sm[1], sc2, nmb));
        RDWO(12);
        u32x4 px = {pk2(pa[0], pa[1]), pk2(pa[2], pa[3]), pk2(pb[0], pb[1]), pk2(pb[2], pb[3])};
        { const unsigned pmp = pk2(pm_0, pm_1); pm0.x = pmp; pm1.y = pmp; }
        asm volatile("" : "+v"(av[0]), "+v"(av[1]));
        { unsigned r0, r1; pack_cols(av[0], av[1], r0, r1); vmt.x = r0; vmt.y = r1; }
        f32x4 o[2];
        asm volatile("s_nop 1" : "+v"(px), "+v"(pm0), "+v"(pm1), "+v"(vmt));
        MF0(o[0], vx[0], px); RDWO(13); SB(); MF0(o[1], vx[1], px); RDWO(14); SB();
        MFA(o[0], vmt, pm0); RDWO(15); SB(); MFA(o[1], vmt, pm1);
        lds_wr8(L_VM + (unsigned)(w * 512) + (l16 >> 1), (u32x2){vmt.x, vmt.y});
        SB();
        const float rl = recip_sum10(pa, pb, pm_0, pm_1);
        SB();
        asm volatile("" : "+v"(o[0]), "+v"(o[1]));
        lds_wr16(owo, scale_pack(o[0], o[1], rl));
        f32x4 pp[4];
#pragma unroll
        for (int tp = 0; tp < 4; ++tp) pp[tp] = (f32x4){0, 0, 0, 0};
        wg_barrier();
        const u32x4 oa0 = lds_rd16(L_O + l16);
        const u32x4 oa1 = lds_rd16(L_O + 1024 + l16);
        asm volatile("s_nop 1" : "+v"(pp[0]), "+v"(pp[1]), "+v"(pp[2]), "+v"(pp[3]));
        {
          const u32x8 w00 = CAT8(wq[0], wq[1]), w01 = CAT8(wq[2], wq[3]), w10 = CAT8(wq[4], wq[5]), w11 = CAT8(wq[6], wq[7]);
          const u32x8 w20 = CAT8(wq[8], wq[9]), w21 = CAT8(wq[10], wq[11]), w30 = CAT8(wq[12], wq[13]), w31 = CAT8(wq[14], wq[15]);
          SMF_V(pp[0], oa0, w00); SMF_V(pp[1], oa0, w10); SMF_V(pp[2], oa0, w20); SMF_V(pp[3], oa0, w30);
          SMF_V(pp[0], oa1, w01); SMF_V(pp[1], oa1, w11); SMF_V(pp[2], oa1, w21); SMF_V(pp[3], oa1, w31); }
        asm volatile("s_nop 5" : "+v"(pp[0]), "+v"(pp[1]));
        u32x4 pv[2];
#pragma unroll
        for (int u = 0; u < 2; ++u) {
            if (u == 1) asm volatile("" : "+v"(pp[2]), "+v"(pp[3]));
            const u32x4 v = part_pack(pp[2 * u], pp[2 * u + 1], 0xFFFFFFFEu);
            pv[u] = v;
            __builtin_amdgcn_raw_buffer_store_b128(v, ws_r, pvo + (unsigned)(u * 4 * 8192), (unsigned)(WS_PART + (size_t)(s % RING) * 64 * 8192), 16);
        }
        SB();
        int ln = lane; asm volatile("" : "+v"(ln));
        if (w == 3) {
            if (nh == 0) {
#pragma unroll
                for (int q2 = 0; q2 < 2; ++q2) {
                    const int ch = q2 * 64 + ln;
                    const u32x4 v = lds_rd16(kxb + 8192 + (unsigned)((((ch >> 5) * 64) + ((ch >> 3) & 3) * 16 + (ch & 7)) * 16));
                    __builtin_amdgcn_raw_buffer_store_b128(v, ws_r, (unsigned)(WS_KM + (size_t)((s % KVR) * 32 + h) * 2048) + ch * 16, 0, 16);
                }
            } else {
#pragma unroll
                for (int q2 = 0; q2 < 2; ++q2) {
                    const u32x4 v = lds_rd16(L_VM + (unsigned)((q2 * 64 + ln) * 16));
                    __builtin_amdgcn_raw_buffer_store_b128(v, ws_r, (unsigned)(WS_VM + (size_t)((s % KVR) * 32 + h) * 2048) + (q2 * 64 + ln) * 16, 0, 16);
                }
            }
            asm volatile("s_waitcnt vmcnt(0)" ::: "memory");
            if (ln == 0) __hip_atomic_store(flags + (nh == 0 ? FL_KM : FL_VM) + ((s % KVR) * 32 + h) * FS, (unsigned)(s + 1), RLX_AGENT);
        }
        if (s == 63) break;
        if (w == 1 || w == 2) {
                        if (__builtin_amdgcn_readfirstlane(kvflag) != (unsigned)(s + 2)) wait_flag(flags + FL_KV + (((s + 1) % KVR) * 32 + h) * FS, (unsigned)(s + 2), tmo);
            for (int d = 0; d < a.gA; ++d) __builtin_amdgcn_s_sleep(1);
            const unsigned src = (unsigned)((w == 1 ? WS_KX : WS_VX) + (size_t)(((s + 1) % KVR) * 32 + h) * 8192);
            const unsigned dst = (w == 1 ? L_KX + (unsigned)((s + 1) & 1) * 12288 : L_VX + (unsigned)((s + 1) & 1) * 8192);
#pragma unroll
            for (int k = 0; k < 8; ++k) dma16(ws_r, src + k * 1024 + ln * 16, dst + k * 1024);
        }
        const unsigned pbase = (unsigned)(WS_PART + (size_t)(s % RING) * 64 * 8192) + (unsigned)b * 8192;
        if (w == 0) {
            for (int d = 0; d < a.dA; ++d) __builtin_amdgcn_s_sleep(1);
            const unsigned loff = (unsigned)((((2 * (ln >> 5) + ((ln >> 3) & 1)) * 2 + ((ln >> 4) & 1)) * 8 + (ln & 7)) * 16);
            const unsigned mdst = (unsigned)(WS_MEMS + (size_t)((s + 1) % MR) * 8192) + (unsigned)(b * 128 + (ln & 31) * 4);
            u32x4 X[8];
            auto ldp = [&](u32x4 (&p)[8], unsigned rep) {
#pragma unroll
                for (int k = 0; k < 8; ++k) p[k] = __builtin_amdgcn_raw_buffer_load_b128(ws_r, loff + k * 1024, pbase + rep * 64 * 8192, 16);
                asm volatile("" ::: "memory");
            };
            auto tryred = [&](u32x4 (&p)[8]) -> bool {
                f32x2 a0 = {__uint_as_float(p[0].x), __uint_as_float(p[0].y)}, a1 = {__uint_as_float(p[0].z), __uint_as_float(p[0].w)};
                f32x2 b0 = {__uint_as_float(p[1].x), __uint_as_float(p[1].y)}, b1 = {__uint_as_float(p[1].z), __uint_as_float(p[1].w)};
#pragma unroll
                for (int k = 2; k < 8; k += 2) {
                    a0 += (f32x2){__uint_as_float(p[k].x), __uint_as_float(p[k].y)}; a1 += (f32x2){__uint_as_float(p[k].z), __uint_as_float(p[k].w)};
                    b0 += (f32x2){__uint_as_float(p[k + 1].x), __uint_as_float(p[k + 1].y)}; b1 += (f32x2){__uint_as_float(p[k + 1].z), __uint_as_float(p[k + 1].w)};
                }
                a0 += b0; a1 += b1;
                float f0 = a0.x, f1 = a0.y, f2 = a1.x, f3 = a1.y;
                f0 += xor8f(f0); f1 += xor8f(f1); f2 += xor8f(f2); f3 += xor8f(f3);
                { float t0 = f0, t1 = f1, t2 = f2, t3 = f3; swap32(f0, t0); swap32(f1, t1); swap32(f2, t2); swap32(f3, t3); f0 += t0; f1 += t1; f2 += t2; f3 += t3; }
                const float chk = (f0 + f1) + (f2 + f3);
                if (__any(chk != chk)) {
                    unsigned mx = 0;
#pragma unroll
                    for (int k = 0; k < 8; ++k) mx = max(mx, max4(p[k]));
                    if (!__all(mx != ~0u)) return false;
                }
                const unsigned d0 = pk2(f0, f1), d1 = pk2(f2, f3);
                const unsigned dv = min((ln & 8) ? d1 : d0, 0xFFFEFFFFu);
                if (ln < 32) __builtin_amdgcn_raw_buffer_store_b32(dv, ws_r, mdst, 0, 16);
                if (ln == 0) lds_wr4(L_FLAG, (unsigned)(s + 1));
                return true;
            };
            for (unsigned spins = 0;; ++spins) {
                ldp(X, 0);
                if (tryred(X)) break;
                if (spins > SPIN_CAP) { __hip_atomic_store(tmo, 2u, RLX_AGENT); if (ln == 0) lds_wr4(L_FLAG, (unsigned)(s + 1)); break; }
            }
        } else {
            for (unsigned spins = 0; lds_rd4(L_FLAG) != (unsigned)(s + 1); ++spins) { asm volatile("" ::: "memory"); __builtin_amdgcn_s_sleep(1); if (spins > SPIN_CAP) break; }
            if (w == 3) {
                const u32x4 pz = {~0u, ~0u, ~0u, ~0u};
#pragma unroll
                for (int k = 0; k < 8; ++k) __builtin_amdgcn_raw_buffer_store_b128(pz, ws_r, (unsigned)(k * 1024 + ln * 16), pbase, 16);
                if (MR < 64 && ln < 8) __builtin_amdgcn_raw_buffer_store_b128(pz, ws_r, (unsigned)(WS_MEMS + (size_t)((s + 3) % MR) * 8192) + (unsigned)(b * 128 + ln * 16), 0, 16);
            }
        }
        ZACC();
        {
            for (int d = 0; d < a.dB; ++d) __builtin_amdgcn_s_sleep(1);
            const unsigned mo = (unsigned)(WS_MEMS + (size_t)((s + 1) % MR) * 8192) + (unsigned)(w * 2048 + ln * 16);
            unsigned an;
            for (unsigned spins = 0;; ++spins) {
                asm volatile("" ::: "memory");
                m0 = __builtin_amdgcn_raw_buffer_load_b128(ws_r, mo, 0, 16);
                m1 = __builtin_amdgcn_raw_buffer_load_b128(ws_r, mo + 1024, 0, 16);
                an = 0;
                if (__all(max(max(max4(m0), max4(m1)), an) != ~0u)) break;
                if (spins > SPIN_CAP) { __hip_atomic_store(tmo, 3u, RLX_AGENT); break; }
                for (int d = 0; d < a.gB; ++d) __builtin_amdgcn_s_sleep(1);
            }
            lds_wr16(L_MEM + (unsigned)(w * 2048 + ln * 16), m0);
            lds_wr16(L_MEM + (unsigned)(w * 2048 + 1024 + ln * 16), m1);
            EARLY();
            if (false) {
                const unsigned target = (unsigned)__builtin_amdgcn_readfirstlane(an) + (unsigned)a.cS;
                for (int it = 0; it < 256 && (int)((unsigned)__builtin_amdgcn_s_memrealtime() - target) < 0; ++it) {}
            }
            wg_barrier();
        }
    }
}

template <int NATURAL, int PIN, int KMODE = 0, int LOWREG = 0>
__device__ __forceinline__ void load_panel(const bf16* Wp, const int ld, u32x4 (&F)[2][16], const int w, const int lane, const unsigned base = 0) {
    const int g = lane >> 4, c = lane & 15;
    u32x4 r[4][8];
    auto ldr = [&](int it) {
        const int kk = 8 * (it >> 1) + 2 * w + (it & 1);
#pragma unroll
        for (int j = 0; j < 8; ++j) r[it][j] = *(const u32x4*)(Wp + (size_t)(KMODE ? (32 * kk + 16 * (j >> 2) + 4 * g + (j & 3)) : (32 * kk + 8 * g + j)) * ld + 8 * c);
    };
    ldr(0); ldr(1);
    if (!LOWREG) { ldr(2); ldr(3); }
#pragma unroll
    for (int hf = 0; hf < 2; ++hf) {
#pragma unroll
        for (int i2 = 0; i2 < 2; ++i2) {
            u32x4 f[8];
            tr8x8(r[2 * hf + i2], f);
#pragma unroll
            for (int t = 0; t < 8; ++t) lds_wr16(base + (unsigned)((t * 514 + (2 * w + i2) * 64 + lane) * 16), f[t]);
        }
        if (LOWREG && hf == 0) { ldr(2); ldr(3); }
        wg_barrier();
#pragma unroll
        for (int tau = 0; tau < 2; ++tau)
#pragma unroll
            for (int k8 = 0; k8 < 8; ++k8) {
                const int T = 2 * w + tau;
                const unsigned ad = base + (NATURAL ? (unsigned)(((c & 7) * 514 + k8 * 64 + g * 16 + 2 * T + (c >> 3)) * 16) : (unsigned)((T * 514 + k8 * 64 + lane) * 16));
                F[tau][8 * hf + k8] = lds_rd16(ad);
                if (PIN) asm volatile("" : "+a"(F[tau][8 * hf + k8]));
            }
        wg_barrier();
    }
}
#define MFD(acc, A_, B_, CA, CB) asm volatile("v_mfma_f32_16x16x32_bf16 %0, %1, %2, %0" : "+v"(acc) : CA(A_), CB(B_))

constexpr unsigned H_XSZ = 32 * 1040;
constexpr unsigned H_X = 0;
constexpr unsigned H_X0 = 0;
constexpr unsigned H_ST0 = H_XSZ;
constexpr unsigned H_KM = H_ST0 + 24576;
constexpr unsigned H_VM = H_KM + 4096;
constexpr unsigned H_X1 = H_VM + 2048;
constexpr unsigned H_ST1 = H_X1 + H_XSZ;
constexpr unsigned H_STG2 = H_X1;
__device__ __forceinline__ unsigned h_x(int buf) { return buf ? H_X1 : H_X0; }
__device__ __forceinline__ unsigned h_st(int i) { return i == 0 ? H_ST0 : H_ST1 + (unsigned)(i - 1) * 24576; }

__device__ __forceinline__ void head_role(const ChainArgs& a, const int u) {
    const int h = u >> 1, par = u & 1;
    const int tid = threadIdx.x, lane = tid & 63, w = __builtin_amdgcn_readfirstlane(tid >> 6);
    const int g = lane >> 4, c = lane & 15;
    const __amdgpu_buffer_rsrc_t ws_r = __builtin_amdgcn_make_buffer_rsrc(a.ws, 0, 0x7fffffff, 0x00020000);
    const __amdgpu_buffer_rsrc_t x_r = __builtin_amdgcn_make_buffer_rsrc((void*)a.x, 0, 2048 * 512 * 2, 0x00020000);
    gu32* flags = (gu32*)(a.ws + WS_FLAG);
    gu32* tmo = flags + FL_TMO;
    u32x4 FK[2][16], FV[2][16], FQ[2][16];
    load_panel<1, 1>(a.Wk + h * 128, 4096, FK, w, lane);
    load_panel<0, 0>(a.Wv + h * 128, 4096, FV, w, lane);
    const float sc2 = 0.08838834764831845f * 1.4426950408889634f;
    auto load_x = [&](int s, int buf) {
#pragma unroll
        for (int r = 0; r < 8; ++r) {
            unsigned keep;
            const unsigned voff = (unsigned)(((s * 32 + 8 * w + r) * 512) * 2 + lane * 16), dst = h_x(buf) + (unsigned)(8 * w + r) * 1040;
            asm volatile("s_mov_b32 %0, m0\n\ts_mov_b32 m0, %3\n\ts_nop 0\n\tbuffer_load_dwordx4 %1, %2, 0 offen lds\n\ts_mov_b32 m0, %0" : "=&s"(keep) : "v"(voff), "s"(x_r), "s"(dst) : "memory");
        }
    };
    auto project = [&](const int i, auto dokv_, auto doq_) {
        constexpr bool DOKV = decltype(dokv_)::value, DOQ = decltype(doq_)::value;
        const int s = 2 * i + par;
        const unsigned xb = h_x(i & 1) + (unsigned)(c * 1040 + g * 16);
        f32x4 dk[2][2], dq[2][2], dv[2][2];
#pragma unroll
        for (int t = 0; t < 2; ++t)
#pragma unroll
            for (int tt = 0; tt < 2; ++tt) { dk[t][tt] = (f32x4){0, 0, 0, 0}; dq[t][tt] = dk[t][tt]; dv[t][tt] = dk[t][tt]; }
        asm volatile("s_nop 1" : "+v"(dk[0][0]), "+v"(dk[0][1]), "+v"(dk[1][0]), "+v"(dk[1][1]), "+v"(dq[0][0]), "+v"(dq[0][1]), "+v"(dq[1][0]), "+v"(dq[1][1]));
        asm volatile("s_nop 1" : "+v"(dv[0][0]), "+v"(dv[0][1]), "+v"(dv[1][0]), "+v"(dv[1][1]));
        u32x4 xq[16][2];
#pragma unroll
        for (int kk = 0; kk < 2; ++kk) { xq[kk][0] = lds_rd16(xb + (unsigned)(kk * 64)); xq[kk][1] = lds_rd16(xb + (unsigned)(16 * 1040 + kk * 64)); }
#pragma unroll
        for (int kk = 0; kk < 16; ++kk) {
            if (kk + 2 < 16) { xq[kk + 2][0] = lds_rd16(xb + (unsigned)((kk + 2) * 64)); xq[kk + 2][1] = lds_rd16(xb + (unsigned)(16 * 1040 + (kk + 2) * 64)); }
            __builtin_amdgcn_sched_barrier(0);
            const u32x4 x0 = xq[kk][0], x1 = xq[kk][1];
#pragma unroll
            for (int t = 0; t < 2; ++t) {
                if constexpr (DOKV) {
                    MFD(dk[t][0], FK[t][kk], x0, "a", "v"); MFD(dk[t][1], FK[t][kk], x1, "a", "v");
                    MFD(dv[t][0], x0, FV[t][kk], "v", "v"); MFD(dv[t][1], x1, FV[t][kk], "v", "v");
                }
                if constexpr (DOQ) { MFD(dq[t][0], FQ[t][kk], x0, "a", "v"); MFD(dq[t][1], FQ[t][kk], x1, "a", "v"); }
            }
        }
        asm volatile("s_nop 7" : "+v"(dk[0][0]), "+v"(dk[0][1]), "+v"(dk[1][0]), "+v"(dk[1][1]), "+v"(dq[0][0]), "+v"(dq[0][1]), "+v"(dq[1][0]), "+v"(dq[1][1]));
        asm volatile("" : "+v"(dv[0][0]), "+v"(dv[0][1]), "+v"(dv[1][0]), "+v"(dv[1][1]));
        const unsigned st = h_st(i % 3);
        const unsigned gk = (unsigned)(WS_KX + (size_t)((s % KVR) * 32 + h) * 8192), gv = (unsigned)(WS_VX + (size_t)((s % KVR) * 32 + h) * 8192);
#pragma unroll
        for (int tt = 0; tt < 2; ++tt) {
            const unsigned o = (unsigned)((tt * 4 + w) * 1024 + lane * 16);
            if constexpr (DOKV) {
                const u32x4 kf = {pk2(dk[0][tt][0], dk[0][tt][1]), pk2(dk[0][tt][2], dk[0][tt][3]), pk2(dk[1][tt][0], dk[1][tt][1]), pk2(dk[1][tt][2], dk[1][tt][3])};
                __builtin_amdgcn_raw_buffer_store_b128(kf, ws_r, gk + o, 0, 16);
                lds_wr16(st + 8192 + o, kf);
            }
            if constexpr (DOQ) {
                const u32x4 qf = {pk2(dq[0][tt][0], dq[0][tt][1]), pk2(dq[0][tt][2], dq[0][tt][3]), pk2(dq[1][tt][0], dq[1][tt][1]), pk2(dq[1][tt][2], dq[1][tt][3])};
                lds_wr16(st + o, qf);
            }
        }
        if constexpr (DOKV) {
#pragma unroll
            for (int t = 0; t < 2; ++t) {
                const u32x4 vf = {pk2(dv[t][0][0], dv[t][0][1]), pk2(dv[t][0][2], dv[t][0][3]), pk2(dv[t][1][0], dv[t][1][1]), pk2(dv[t][1][2], dv[t][1][3])};
                const unsigned o = (unsigned)((2 * w + t) * 1024 + lane * 16);
                __builtin_amdgcn_raw_buffer_store_b128(vf, ws_r, gv + o, 0, 16);
                lds_wr16(st + 16384 + o, vf);
            }
            asm volatile("s_waitcnt vmcnt(0)" ::: "memory");
            wg_barrier();
            if (tid == 0) __hip_atomic_store(flags + FL_KV + ((s % KVR) * 32 + h) * FS, (unsigned)(s + 1), RLX_AGENT);
        }
    };
    typedef std::integral_constant<bool, true> T_; typedef std::integral_constant<bool, false> F_;
    load_x(par, 0);
    asm volatile("s_waitcnt vmcnt(0)" ::: "memory");
    wg_barrier();
    project(0, T_{}, F_{});
    load_panel<1, 1, 0, 1>(a.Wq + h * 128, 4096, FQ, w, lane, H_STG2);
    load_x(par + 2, 1);
    project(0, F_{}, T_{});
    for (int i = 1; i < 32 + 2; ++i) {
        if (i < 32) {
            asm volatile("s_waitcnt vmcnt(0)" ::: "memory");
            wg_barrier();
            if (i + 1 < 32) load_x(2 * (i + 1) + par, (i + 1) & 1);
            project(i, T_{}, T_{});
        }
        if (i >= 2 && a.pad == 0) {
            const int s = 2 * (i - 2) + par;
            const unsigned st = h_st((i - 2) % 3);
            if (w == 0) {
                wait_flag(flags + FL_KM + ((s % KVR) * 32 + h) * FS, (unsigned)(s + 1), tmo);
#pragma unroll
                for (int q2 = 0; q2 < 2; ++q2) {
                    const int ch = q2 * 64 + lane;
                    const u32x4 v = __builtin_amdgcn_raw_buffer_load_b128(ws_r, (unsigned)(WS_KM + (size_t)((s % KVR) * 32 + h) * 2048) + ch * 16, 0, 16);
                    lds_wr16(H_KM + (unsigned)((((ch >> 5) * 64) + ((ch >> 3) & 3) * 16 + (ch & 7)) * 16), v);
                }
            } else if (w == 1) {
                wait_flag(flags + FL_VM + ((s % KVR) * 32 + h) * FS, (unsigned)(s + 1), tmo);
#pragma unroll
                for (int q2 = 0; q2 < 2; ++q2) {
                    const u32x4 v = __builtin_amdgcn_raw_buffer_load_b128(ws_r, (unsigned)(WS_VM + (size_t)((s % KVR) * 32 + h) * 2048) + (q2 * 64 + lane) * 16, 0, 16);
                    lds_wr16(H_VM + (unsigned)((q2 * 64 + lane) * 16), v);
                }
            }
            wg_barrier();
            const int qt = w & 1, eh = w >> 1;
            f32x4 Sm, Sa, Sb;
            const unsigned km = H_KM + (unsigned)((g * 16 + 2 * (c >> 2) + (c & 1)) * 16);
            {
                u32x4 q8[4], kmf[4], kaf[4], kbf[4];
#pragma unroll
                for (int ks = 0; ks < 4; ++ks) {
                    q8[ks] = lds_rd16(st + (unsigned)((qt * 4 + ks) * 1024 + lane * 16));
                    kmf[ks] = lds_rd16(km + ks * 1024);
                    kaf[ks] = lds_rd16(st + 8192 + (unsigned)(ks * 1024 + lane * 16));
                    kbf[ks] = lds_rd16(st + 8192 + (unsigned)((4 + ks) * 1024 + lane * 16));
                }
                MF0(Sm, kmf[0], q8[0]); MF0(Sa, kaf[0], q8[0]); MF0(Sb, kbf[0], q8[0]);
#pragma unroll
                for (int ks = 1; ks < 4; ++ks) { MFA(Sm, kmf[ks], q8[ks]); MFA(Sa, kaf[ks], q8[ks]); MFA(Sb, kbf[ks], q8[ks]); }
                asm volatile("s_nop 7" : "+v"(Sm), "+v"(Sa), "+v"(Sb));
            }
            const float NEG = -3.0e38f;
            float sa[4], sb[4];
#pragma unroll
            for (int r = 0; r < 4; ++r) {
                const bool diag = (4 * g + r) <= c;
                sa[r] = (qt == 1 || diag) ? Sa[r] : NEG;
                sb[r] = (qt == 1 && diag) ? Sb[r] : NEG;
            }
            float m = fmaxf(fmaxf(fmaxf(sa[0], sa[1]), fmaxf(sa[2], sa[3])), fmaxf(fmaxf(sb[0], sb[1]), fmaxf(sb[2], sb[3])));
            m = fmaxf(m, fmaxf(Sm[0], Sm[1]));
            m = fmaxf(m, xor16f(m)); m = fmaxf(m, xor32f(m));
            const float nm = -m * sc2;
            float pa[4], pb[4];
#pragma unroll
            for (int r = 0; r < 4; ++r) {
                pa[r] = sa[r] == NEG ? 0.f : __builtin_amdgcn_exp2f(__builtin_fmaf(sa[r], sc2, nm));
                pb[r] = sb[r] == NEG ? 0.f : __builtin_amdgcn_exp2f(__builtin_fmaf(sb[r], sc2, nm));
            }
            const float pm0 = __builtin_amdgcn_exp2f(__builtin_fmaf(Sm[0], sc2, nm)), pm1 = __builtin_amdgcn_exp2f(__builtin_fmaf(Sm[1], sc2, nm));
            float l = ((pa[0] + pa[1]) + (pa[2] + pa[3])) + ((pb[0] + pb[1]) + (pb[2] + pb[3])) + (pm0 + pm1);
            l += xor16f(l); l += xor32f(l);
            const float rl = __builtin_amdgcn_rcpf(l);
            u32x4 px = {pk2(pa[0] * rl, pa[1] * rl), pk2(pa[2] * rl, pa[3] * rl), pk2(pb[0] * rl, pb[1] * rl), pk2(pb[2] * rl, pb[3] * rl)};
            u32x4 pmv = {pk2(pm0 * rl, pm1 * rl), 0u, 0u, 0u};
            f32x4 o[4];
#pragma unroll
            for (int tq = 0; tq < 4; ++tq) {
                const int t = 4 * eh + tq;
                const u32x4 vx = lds_rd16(st + 16384 + (unsigned)(t * 1024 + lane * 16));
                const unsigned vm = lds_rd4(H_VM + (unsigned)(((t >> 1) * 64 + lane) * 8 + (t & 1) * 4));
                u32x4 va = {vm, 0u, 0u, 0u};
                asm volatile("s_nop 1" : "+v"(va), "+v"(px), "+v"(pmv));
                MF0(o[tq], vx, px);
                MFA(o[tq], va, pmv);
            }
            asm volatile("s_nop 7" : "+v"(o[0]), "+v"(o[1]), "+v"(o[2]), "+v"(o[3]));
            const unsigned go = (unsigned)(WS_OX + (size_t)((s * 2 + qt) * 128 + h * 4 + eh * 2) * 1024) + (unsigned)lane * 16;
#pragma unroll
            for (int f = 0; f < 2; ++f) {
                const u32x4 v = {pk2(o[0][2 * f], o[1][2 * f]), pk2(o[2][2 * f], o[3][2 * f]), pk2(o[0][2 * f + 1], o[1][2 * f + 1]), pk2(o[2][2 * f + 1], o[3][2 * f + 1])};
                __builtin_amdgcn_raw_buffer_store_b128(v, ws_r, go + f * 1024, 0, 16);
            }
            asm volatile("s_waitcnt vmcnt(0)" ::: "memory");
            wg_barrier();
            if (tid == 0) __hip_atomic_fetch_add(flags + FL_O + s * FS, 1u, RLX_AGENT);
        }
    }
}

constexpr int NREP = 3;
__device__ __forceinline__ void oproj_role(const ChainArgs& a, const int u) {
    const int n = u & 15, rp = u >> 4;
    const int tid = threadIdx.x, lane = tid & 63, w = __builtin_amdgcn_readfirstlane(tid >> 6);
    const int g = lane >> 4, c = lane & 15;
    const __amdgpu_buffer_rsrc_t ws_r = __builtin_amdgcn_make_buffer_rsrc(a.ws, 0, 0x7fffffff, 0x00020000);
    gu32* flags = (gu32*)(a.ws + WS_FLAG);
    gu32* tmo = flags + FL_TMO;
    u32x4 F[32][2];
    {
        const int kq = lane >> 4, gg = (lane >> 2) & 3, oct = lane & 3;
        const unsigned lb = (unsigned)w * 16384;
#pragma unroll
        for (int it = 0; it < 8; ++it) {
            const int kk = 32 * w + 4 * it + kq;
            const int hh = kk >> 2, eh = (kk >> 1) & 1, f = kk & 1;
            const bf16* Wp = a.Wo + (size_t)(hh * 128 + 32 * gg + 16 * f + 4 * eh) * 512 + 32 * n + 8 * oct;
            u32x4 r[8], fr[8];
#pragma unroll
            for (int j = 0; j < 8; ++j) r[j] = *(const u32x4*)(Wp + (size_t)(8 * (j >> 2) + (j & 3)) * 512);
            tr8x8(r, fr);
#pragma unroll
            for (int t = 0; t < 8; ++t) lds_wr16(lb + (unsigned)((((kq * 2 + (oct >> 1)) * 4 + gg) * 16 + 8 * (oct & 1) + t) * 16), fr[t]);
            asm volatile("s_waitcnt lgkmcnt(0)" ::: "memory");
#pragma unroll
            for (int q4 = 0; q4 < 4; ++q4)
#pragma unroll
                for (int T = 0; T < 2; ++T) { F[4 * it + q4][T] = lds_rd16(lb + (unsigned)(((q4 * 2 + T) * 64 + lane) * 16)); asm volatile("" : "+a"(F[4 * it + q4][T])); }
            asm volatile("s_waitcnt lgkmcnt(0)" ::: "memory");
        }
    }
    wg_barrier();
    for (int un = 0; un < 128; ++un) {
                if ((un < 32 ? un % NREP : un % 4) != rp) continue;
        const int s = un >> 1, rh = un & 1;
        if (w == 0) {
            gu32* f = flags + FL_O + s * FS;
            for (unsigned spins = 0;; ++spins) {
                const unsigned v = __builtin_amdgcn_readfirstlane(__hip_atomic_load(f, RLX_AGENT));
                if (v == 31u) break;
                if (spins > SPIN_CAP) { __hip_atomic_store(tmo, 4u, RLX_AGENT); break; }
                __builtin_amdgcn_s_sleep(PSLEEP * 2);
            }
        }
        wg_barrier();
        const unsigned ab = (unsigned)(WS_OX + (size_t)((s * 2 + rh) * 128 + 32 * w) * 1024) + (unsigned)lane * 16;
        f32x4 d0 = {0, 0, 0, 0}, d1 = d0;
        asm volatile("s_nop 1" : "+v"(d0), "+v"(d1));
#pragma unroll
        for (int hb = 0; hb < 2; ++hb) {
            u32x4 A[16];
#pragma unroll
            for (int i = 0; i < 16; ++i) A[i] = __builtin_amdgcn_raw_buffer_load_b128(ws_r, ab + (unsigned)((16 * hb + i) * 1024), 0, 16);
#pragma unroll
            for (int i = 0; i < 16; ++i) { MFD(d0, A[i], F[16 * hb + i][0], "v", "a"); MFD(d1, A[i], F[16 * hb + i][1], "v", "a"); }
        }
        asm volatile("s_nop 7" : "+v"(d0), "+v"(d1));
        lds_wr16(65536u + (unsigned)(((w * 2 + 0) * 64 + lane) * 16), __builtin_bit_cast(u32x4, d0));
        lds_wr16(65536u + (unsigned)(((w * 2 + 1) * 64 + lane) * 16), __builtin_bit_cast(u32x4, d1));
        wg_barrier();
        {
            const int tok = tid >> 4, cp = tid & 15, T = cp >> 3, c0 = (2 * cp) & 15;
            float v0 = 0, v1 = 0;
#pragma unroll
            for (int ww = 0; ww < 4; ++ww) {
                const unsigned ad = 65536u + (unsigned)((((ww * 2 + T) * 64) + (tok >> 2) * 16 + c0) * 16 + (tok & 3) * 4);
                v0 += __uint_as_float(lds_rd4(ad)); v1 += __uint_as_float(lds_rd4(ad + 16));
            }
            *(unsigned*)(a.out + (size_t)(s * 32 + rh * 16 + tok) * 1024 + 512 + 32 * n + 2 * cp) = pk2(v0, v1);
        }
        wg_barrier();
    }
}

__device__ __forceinline__ void wait_count(gu32* f, unsigned want, gu32* tmo) {
    for (unsigned spins = 0;; ++spins) {
        const unsigned v = __builtin_amdgcn_readfirstlane(__hip_atomic_load(f, RLX_AGENT));
        if (v == want) return;
        if (spins > SPIN_CAP) { __hip_atomic_store(tmo, 5u, RLX_AGENT); return; }
        __builtin_amdgcn_s_sleep(8);
    }
}
template <int NV>
__device__ __forceinline__ void a1_unit(const ChainArgs& a, const int j, const int rg) {
    const int tid = threadIdx.x, lane = tid & 63, w = __builtin_amdgcn_readfirstlane(tid >> 6);
    const int g = lane >> 4, c = lane & 15;
    const __amdgpu_buffer_rsrc_t ws_r = __builtin_amdgcn_make_buffer_rsrc(a.ws, 0, 0x7fffffff, 0x00020000);
    const __amdgpu_buffer_rsrc_t x_r = __builtin_amdgcn_make_buffer_rsrc((void*)a.ax, 0, 2048 * 512 * 2, 0x00020000);
    u32x4 F0[2][16], F1[2][16], F2[2][16];
    load_panel<1, 1>(a.awqkv + 384 * j, 1536, F0, w, lane);
    load_panel<1, 1>(a.awqkv + 384 * j + 128, 1536, F1, w, lane);
    load_panel<1, 0, 0, 1>(a.awqkv + 384 * j + 256, 1536, F2, w, lane);
    auto load_x = [&](int t32, int buf) {
#pragma unroll
        for (int r = 0; r < 8; ++r) {
            unsigned keep;
            const unsigned voff = (unsigned)(((t32 * 32 + 8 * w + r) * 512) * 2 + lane * 16), dst = h_x(buf) + (unsigned)(8 * w + r) * 1040;
            asm volatile("s_mov_b32 %0, m0\n\ts_mov_b32 m0, %3\n\ts_nop 0\n\tbuffer_load_dwordx4 %1, %2, 0 offen lds\n\ts_mov_b32 m0, %0" : "=&s"(keep) : "v"(voff), "s"(x_r), "s"(dst) : "memory");
        }
    };
    load_x(rg * 4, 0);
    for (int i = 0; i < 4; ++i) {
        asm volatile("s_waitcnt vmcnt(0)" ::: "memory");
        wg_barrier();
        if (i + 1 < 4) load_x(rg * 4 + i + 1, (i + 1) & 1);
        const unsigned xb = h_x(i & 1) + (unsigned)(c * 1040 + g * 16);
        f32x4 d[3][2][2];
#pragma unroll
        for (int p = 0; p < 3; ++p)
#pragma unroll
            for (int t = 0; t < 2; ++t) { d[p][t][0] = (f32x4){0, 0, 0, 0}; d[p][t][1] = d[p][t][0]; }
        asm volatile("s_nop 1" : "+v"(d[0][0][0]), "+v"(d[0][0][1]), "+v"(d[0][1][0]), "+v"(d[0][1][1]), "+v"(d[1][0][0]), "+v"(d[1][0][1]), "+v"(d[1][1][0]), "+v"(d[1][1][1]));
        asm volatile("s_nop 1" : "+v"(d[2][0][0]), "+v"(d[2][0][1]), "+v"(d[2][1][0]), "+v"(d[2][1][1]));
#pragma unroll
        for (int kk = 0; kk < 16; ++kk) {
            const u32x4 x0 = lds_rd16(xb + (unsigned)(kk * 64)), x1 = lds_rd16(xb + (unsigned)(16 * 1040 + kk * 64));
#pragma unroll
            for (int t = 0; t < 2; ++t) {
                if (NV >= 3) { MFD(d[0][t][0], x0, F0[t][kk], "v", "a"); MFD(d[0][t][1], x1, F0[t][kk], "v", "a"); MFD(d[1][t][0], x0, F1[t][kk], "v", "a"); MFD(d[1][t][1], x1, F1[t][kk], "v", "a"); }
                else { MFD(d[0][t][0], F0[t][kk], x0, "a", "v"); MFD(d[0][t][1], F0[t][kk], x1, "a", "v"); MFD(d[1][t][0], F1[t][kk], x0, "a", "v"); MFD(d[1][t][1], F1[t][kk], x1, "a", "v"); }
                if (NV >= 1) { MFD(d[2][t][0], x0, F2[t][kk], "v", "v"); MFD(d[2][t][1], x1, F2[t][kk], "v", "v"); }
                else { MFD(d[2][t][0], F2[t][kk], x0, "v", "v"); MFD(d[2][t][1], F2[t][kk], x1, "v", "v"); }
            }
        }
        asm volatile("s_nop 7" : "+v"(d[0][0][0]), "+v"(d[0][0][1]), "+v"(d[0][1][0]), "+v"(d[0][1][1]), "+v"(d[1][0][0]), "+v"(d[1][0][1]), "+v"(d[1][1][0]), "+v"(d[1][1][1]));
        asm volatile("" : "+v"(d[2][0][0]), "+v"(d[2][0][1]), "+v"(d[2][1][0]), "+v"(d[2][1][1]));
#pragma unroll
        for (int p = 0; p < 3; ++p)
#pragma unroll
            for (int t = 0; t < 2; ++t)
#pragma unroll
                for (int tt = 0; tt < 2; ++tt) {
                    const int hd = (384 * j + 128 * p) / 16 + 2 * w + t;
                    const int tile = (rg * 4 + i) * 2 + tt;
                    const u32x2 v = {pk2(d[p][t][tt][0], d[p][t][tt][1]), pk2(d[p][t][tt][2], d[p][t][tt][3])};
                    __builtin_amdgcn_raw_buffer_store_b64(v, ws_r, (unsigned)(WS_AQ + (size_t)(hd * 128 + tile) * 512) + (unsigned)lane * 8, 0, 16);
                }
    }
    asm volatile("s_waitcnt vmcnt(0)" ::: "memory");
    wg_barrier();
    if (tid == 0) __hip_atomic_fetch_add((gu32*)(a.ws + WS_FLAG) + FL_A1 + rg * FS, 1u, RLX_AGENT);
}

__device__ __forceinline__ void a2_unit(const ChainArgs& a, const int un) {
    const int rg64 = un >> 2, ho = un & 3;
    const int tid = threadIdx.x, lane = tid & 63, w = __builtin_amdgcn_readfirstlane(tid >> 6);
    const int g = lane >> 4, c = lane & 15;
    const __amdgpu_buffer_rsrc_t ws_r = __builtin_amdgcn_make_buffer_rsrc(a.ws, 0, 0x7fffffff, 0x00020000);
    gu32* flags = (gu32*)(a.ws + WS_FLAG);
    if (w == 0) {
        const int lo = (64 * rg64 - 256) < 0 ? 0 : (64 * rg64 - 256) / 128, hi = (64 * rg64 + 63) / 128;
        for (int r = lo; r <= hi; ++r) wait_count(flags + FL_A1 + r * FS, 3u, flags + FL_TMO);
    }
    wg_barrier();
    const float sc2 = 0.25f * 1.4426950408889634f;
    for (int it = 0; it < 8; ++it) {
        const int hd = 8 * ho + 2 * w + (it >> 2), qt = 4 * rg64 + (it & 3);
        const unsigned qb = (unsigned)(WS_AQ + (size_t)(hd * 128) * 512) + (unsigned)lane * 8;
        const unsigned kb = qb + 32u * 128u * 512u, vb = kb + 32u * 128u * 512u;
        const u32x2 qf = __builtin_amdgcn_raw_buffer_load_b64(ws_r, qb + (unsigned)qt * 512, 0, 16);
        u32x2 kf[17], vf[17];
#pragma unroll
        for (int i = 0; i < 17; ++i) {
            const int kt = qt - 16 + i, ktc = kt < 0 ? 0 : kt;
            kf[i] = __builtin_amdgcn_raw_buffer_load_b64(ws_r, kb + (unsigned)ktc * 512, 0, 16);
            vf[i] = __builtin_amdgcn_raw_buffer_load_b64(ws_r, vb + (unsigned)ktc * 512, 0, 16);
        }
        f32x4 S[17];
#pragma unroll
        for (int i = 0; i < 17; ++i) {
            f32x4 z = {0, 0, 0, 0};
            S[i] = __builtin_amdgcn_mfma_f32_16x16x16bf16_1k(__builtin_bit_cast(bf16x4, kf[i]), __builtin_bit_cast(bf16x4, qf), z, 0, 0, 0);
        }
        const float NEG = -3.0e38f;
        float m = NEG;
#pragma unroll
        for (int i = 0; i < 17; ++i) {
            const int kt = qt - 16 + i;
#pragma unroll
            for (int r = 0; r < 4; ++r) {
                bool ok = kt >= 0;
                if (i == 0) ok = ok && (4 * g + r) > c;
                if (i == 16) ok = (4 * g + r) <= c;
                S[i][r] = ok ? S[i][r] : NEG;
                m = fmaxf(m, S[i][r]);
            }
        }
        m = fmaxf(m, xor16f(m)); m = fmaxf(m, xor32f(m));
        const float nm = -m * sc2;
        float l = 0;
        u32x2 P[18];
#pragma unroll
        for (int i = 0; i < 17; ++i) {
            float p[4];
#pragma unroll
            for (int r = 0; r < 4; ++r) { p[r] = S[i][r] == NEG ? 0.f : __builtin_amdgcn_exp2f(__builtin_fmaf(S[i][r], sc2, nm)); l += p[r]; }
            P[i] = (u32x2){pk2(p[0], p[1]), pk2(p[2], p[3])};
        }
        P[17] = (u32x2){0u, 0u};
        l += xor16f(l); l += xor32f(l);
        f32x4 o = {0, 0, 0, 0};
#pragma unroll
        for (int i = 0; i < 9; ++i) {
            const u32x2 v1 = (2 * i + 1 < 17) ? vf[2 * i + 1 < 17 ? 2 * i + 1 : 0] : (u32x2){0u, 0u};
            const u32x4 va = {vf[2 * i].x, vf[2 * i].y, v1.x, v1.y};
            const u32x4 pbv = {P[2 * i].x, P[2 * i].y, P[2 * i + 1].x, P[2 * i + 1].y};
            o = __builtin_amdgcn_mfma_f32_16x16x32_bf16(__builtin_bit_cast(bf16x8, va), __builtin_bit_cast(bf16x8, pbv), o, 0, 0, 0);
        }
        const float rl = __builtin_amdgcn_rcpf(l);
        const u32x2 ov = {pk2(o[0] * rl, o[1] * rl), pk2(o[2] * rl, o[3] * rl)};
        __builtin_amdgcn_raw_buffer_store_b64(ov, ws_r, (unsigned)(WS_AO + (size_t)(qt * 16 + (hd >> 1)) * 1024) + (unsigned)(lane * 16 + (hd & 1) * 8), 0, 16);
    }
    asm volatile("s_waitcnt vmcnt(0)" ::: "memory");
    wg_barrier();
    if (tid == 0) __hip_atomic_fetch_add(flags + FL_A2 + rg64 * FS, 1u, RLX_AGENT);
}

__device__ __forceinline__ void a3_unit(const ChainArgs& a, const int un) {
    const int js = un & 1, rgp = un >> 1;
    const int tid = threadIdx.x, lane = tid & 63, w = __builtin_amdgcn_readfirstlane(tid >> 6);
    const int g = lane >> 4, c = lane & 15;
    const __amdgpu_buffer_rsrc_t ws_r = __builtin_amdgcn_make_buffer_rsrc(a.ws, 0, 0x7fffffff, 0x00020000);
    gu32* flags = (gu32*)(a.ws + WS_FLAG);
    u32x4 F0[2][16], F1[2][16];
    load_panel<1, 1, 1>(a.awo + 256 * js, 512, F0, w, lane);
    load_panel<1, 1, 1>(a.awo + 256 * js + 128, 512, F1, w, lane);
    if (w == 0) { wait_count(flags + FL_A2 + (2 * rgp) * FS, 3u, flags + FL_TMO); wait_count(flags + FL_A2 + (2 * rgp + 1) * FS, 3u, flags + FL_TMO); }
    wg_barrier();
    for (int i = 0; i < 8; ++i) {
        const int tile = rgp * 8 + i;
        u32x4 A[16];
#pragma unroll
        for (int kk = 0; kk < 16; ++kk) A[kk] = __builtin_amdgcn_raw_buffer_load_b128(ws_r, (unsigned)(WS_AO + (size_t)(tile * 16 + kk) * 1024) + (unsigned)lane * 16, 0, 16);
        f32x4 d[2][2];
        d[0][0] = (f32x4){0, 0, 0, 0}; d[0][1] = d[0][0]; d[1][0] = d[0][0]; d[1][1] = d[0][0];
        asm volatile("s_nop 1" : "+v"(d[0][0]), "+v"(d[0][1]), "+v"(d[1][0]), "+v"(d[1][1]));
#pragma unroll
        for (int kk = 0; kk < 16; ++kk)
#pragma unroll
            for (int t = 0; t < 2; ++t) { MFD(d[0][t], F0[t][kk], A[kk], "a", "v"); MFD(d[1][t], F1[t][kk], A[kk], "a", "v"); }
        asm volatile("s_nop 7" : "+v"(d[0][0]), "+v"(d[0][1]), "+v"(d[1][0]), "+v"(d[1][1]));
#pragma unroll
        for (int p = 0; p < 2; ++p)
#pragma unroll
            for (int t = 0; t < 2; ++t) {
                const u32x2 v = {pk2(d[p][t][0], d[p][t][1]), pk2(d[p][t][2], d[p][t][3])};
                *(u32x2*)(a.out + (size_t)(tile * 16 + c) * 1024 + 256 * js + 128 * p + 16 * (2 * w + t) + 4 * g) = v;
            }
    }
}

__device__ __forceinline__ void a_role(const ChainArgs& a, const int ua) {
    if (ua < 64) {
        const int j = ua & 3, rg = ua >> 2;
        if (j < 2) a1_unit<0>(a, j, rg); else if (j == 2) a1_unit<1>(a, j, rg); else a1_unit<3>(a, j, rg);
    }
    wg_barrier();
    for (int un = ua; un < 128; un += 80) a2_unit(a, un);
    wg_barrier();
    if (ua >= 48) a3_unit(a, ua - 48);
}

__global__ void __launch_bounds__(256, 1) fused_kernel(ChainArgs a) {
    extern __shared__ unsigned char smem[];
    const int b = blockIdx.x;
    const int x = b & 7, slot = b >> 3;
    if (x < 4) {
        if (slot < 16) chain_role(a, x * 16 + slot);
        else a_role(a, x * 16 + (slot - 16));
    } else {
        if (slot < 16) head_role(a, (x - 4) * 16 + slot);
        else if (x < 7) oproj_role(a, (x - 4) * 16 + (slot - 16));
        else { a_role(a, 64 + (slot - 16)); wg_barrier(); oproj_role(a, 3 * 16 + (slot - 16)); }
    }
}

#define STOP 1000
#define CS 60
#define GA 0
#define GB 0
#define DA 9
#define DB 8
extern "C" void kernel_launch(void* const* d_in, const int* in_sizes, int n_in, void* d_out, int out_size, void* d_ws, size_t ws_size, hipStream_t stream) {
    const bf16* Ax = (const bf16*)d_in[0]; const bf16* Awqkv = (const bf16*)d_in[1]; const bf16* Awo = (const bf16*)d_in[2];
    const bf16* Bx = (const bf16*)d_in[3]; const bf16* mem0 = (const bf16*)d_in[4];
    const bf16* Wq = (const bf16*)d_in[5]; const bf16* Wk = (const bf16*)d_in[6]; const bf16* Wv = (const bf16*)d_in[7]; const bf16* Wo = (const bf16*)d_in[8];
    bf16* out = (bf16*)d_out;
    unsigned char* wsb = (unsigned char*)d_ws;
    bf16* ws = (bf16*)(wsb + WS_END1);
    bf16* qkv = ws; ws += (size_t)2048 * 1536;
    bf16* oa = ws; ws += (size_t)2048 * 512;
    (void)hipMemsetAsync(wsb, 0xFF, WS_INIT_BYTES, stream);
    (void)hipFuncSetAttribute((const void*)fused_kernel, hipFuncAttributeMaxDynamicSharedMemorySize, LDS_BYTES);
    ChainArgs ca{Bx, out, Ax, Awqkv, Awo, mem0, Wq, Wk, Wv, Wo, wsb, DA, DB, GA, GB, STOP, 0, CS, 0};
    fused_kernel<<<256, 256, LDS_BYTES, stream>>>(ca);
}
```

```cpp
#include <hip/hip_runtime.h>
#include <hip/hip_bf16.h>
#include <stdint.h>
#include <type_traits>

typedef __hip_bfloat16 bf16;
typedef __bf16 bf16x8 __attribute__((ext_vector_type(8)));
typedef __bf16 bf16x4 __attribute__((ext_vector_type(4)));
typedef __bf16 bf16x2 __attribute__((ext_vector_type(2)));
typedef __bf16 bf16x16 __attribute__((ext_vector_type(16)));
typedef float f32x4 __attribute__((ext_vector_type(4)));
typedef float f32x2 __attribute__((ext_vector_type(2)));
typedef unsigned u32x4 __attribute__((ext_vector_type(4)));
typedef unsigned u32x2 __attribute__((ext_vector_type(2)));
typedef unsigned u32x8 __attribute__((ext_vector_type(8)));
#define LAS __attribute__((address_space(3)))
#define GAS __attribute__((address_space(1)))
typedef GAS unsigned gu32;
#define RLX_AGENT __ATOMIC_RELAXED, __HIP_MEMORY_SCOPE_AGENT

constexpr int PSLEEP = 2;
constexpr int RING = 2;
constexpr int NVREG = 6;
constexpr int KVR = 8;
constexpr int MR = 64;
constexpr int FS = 32;
constexpr int FL_KV = 0;
constexpr int FL_KM = FL_KV + KVR * 32 * FS;
constexpr int FL_VM = FL_KM + KVR * 32 * FS;
constexpr int FL_O = FL_VM + KVR * 32 * FS;
constexpr int FL_A1 = FL_O + 64 * FS;
constexpr int FL_A2 = FL_A1 + 16 * FS;
constexpr int FL_RDY = FL_A2 + 32 * FS;
constexpr int FL_TMO = FL_RDY + FS;
constexpr int FL_END = FL_TMO + FS;
constexpr size_t WS_FLAG = 0;
constexpr size_t WS_MEMS = WS_FLAG + (size_t)FL_END * 4;
constexpr size_t WS_INIT_BYTES = WS_MEMS + (size_t)MR * 8192;
constexpr size_t WS_PART = (WS_INIT_BYTES + 4095) / 4096 * 4096;
constexpr size_t WS_KX = WS_PART + (size_t)RING * 64 * 8192;
constexpr size_t WS_VX = WS_KX + (size_t)KVR * 32 * 8192;
constexpr size_t WS_KM = WS_VX + (size_t)KVR * 32 * 8192;
constexpr size_t WS_VM = WS_KM + (size_t)KVR * 32 * 2048;
constexpr size_t WS_OX = WS_VM + (size_t)KVR * 32 * 2048;
constexpr size_t WS_AQ = WS_OX + (size_t)64 * 2 * 128 * 1024;
constexpr size_t WS_AO = WS_AQ + (size_t)3 * 32 * 128 * 512;
constexpr size_t WS_END1 = WS_AO + (size_t)128 * 16 * 1024;
static_assert(WS_INIT_BYTES % 16 == 0, "memset size");

constexpr int DELAY_A = 9, DELAY_B = 8;
constexpr unsigned SPIN_CAP = 1u << 20;

constexpr unsigned L_MEM = 0;
constexpr unsigned L_KX = 8192;
constexpr unsigned L_VX = L_KX + 2 * 12288;
constexpr unsigned L_Q = L_VX + 2 * 8192;
constexpr unsigned L_O = L_Q + 4096;
constexpr unsigned L_VM = L_O + 2048;
constexpr unsigned L_FLAG = L_VM + 2048;
constexpr unsigned L_DUMMY = L_FLAG + 64;
constexpr unsigned L_WV = 65536;
constexpr unsigned L_WO = 98304;
constexpr unsigned L_STG = 0;
constexpr unsigned LDS_BYTES = 160 * 1024;

__device__ __forceinline__ u32x4 lds_rd16(unsigned a) { return *(LAS u32x4*)(size_t)a; }
__device__ __forceinline__ void lds_wr16(unsigned a, u32x4 v) { *(LAS u32x4*)(size_t)a = v; }
__device__ __forceinline__ void lds_wr4(unsigned a, unsigned v) { *(LAS unsigned*)(size_t)a = v; }
__device__ __forceinline__ void lds_wr8(unsigned a, u32x2 v) { *(LAS u32x2*)(size_t)a = v; }
__device__ __forceinline__ unsigned lds_rd4(unsigned a) { return *(LAS unsigned*)(size_t)a; }
__device__ __forceinline__ void wg_barrier() { asm volatile("s_waitcnt lgkmcnt(0)" ::: "memory"); __builtin_amdgcn_s_barrier(); asm volatile("" ::: "memory"); }
__device__ __forceinline__ unsigned pk2(float a, float b) { bf16x2 v = {(__bf16)a, (__bf16)b}; return __builtin_bit_cast(unsigned, v); }
__device__ __forceinline__ unsigned max4(u32x4 a) { return max(max(a.x, a.y), max(a.z, a.w)); }
__device__ __forceinline__ float xor8f(float v) { return __builtin_bit_cast(float, __builtin_amdgcn_update_dpp(0, __builtin_bit_cast(int, v), 0x128  , 0xf, 0xf, false)); }
__device__ __forceinline__ float xor16f(float v) {
    auto r = __builtin_amdgcn_permlane16_swap(__builtin_bit_cast(unsigned, v), __builtin_bit_cast(unsigned, v), false, false);
    return __builtin_bit_cast(float, (threadIdx.x & 16) ? r[0] : r[1]);
}
__device__ __forceinline__ float xor32f(float v) {
    auto r = __builtin_amdgcn_permlane32_swap(__builtin_bit_cast(unsigned, v), __builtin_bit_cast(unsigned, v), false, false);
    return __builtin_bit_cast(float, (threadIdx.x & 32) ? r[0] : r[1]);
}

__device__ __forceinline__ void tr8x8(const u32x4 (&r)[8], u32x4 (&f)[8]) {
#pragma unroll
    for (int t = 0; t < 8; ++t) {
        const unsigned sel = (t & 1) ? 0x07060302u : 0x05040100u;
#pragma unroll
        for (int d = 0; d < 4; ++d) f[t][d] = __builtin_amdgcn_perm(r[2 * d + 1][t >> 1], r[2 * d][t >> 1], sel);
    }
}

#define SMF_A(acc, A_, W_) asm volatile("v_smfmac_f32_16x16x64_bf16 %0, %1, %2, %3" : "+v"(acc) : "v"(A_), "a"(W_), "v"(idx))
#define SMF_V(acc, A_, W_) asm volatile("v_smfmac_f32_16x16x64_bf16 %0, %1, %2, %3" : "+v"(acc) : "v"(A_), "v"(W_), "v"(idx))
#define SMF_VN(acc, A_, W_) asm volatile("s_nop 1\n\tv_smfmac_f32_16x16x64_bf16 %0, %1, %2, %3" : "+v"(acc) : "v"(A_), "v"(W_), "v"(idx))
__device__ __forceinline__ void swap32(float& a, float& b) { asm volatile("s_nop 1\n\tv_permlane32_swap_b32 %0, %1" : "+v"(a), "+v"(b)); }
__device__ __forceinline__ void pack_rows(const f32x4 a, const f32x4 b, unsigned& r0, unsigned& r1) {
    float t0, t1, t2, t3;
    asm("v_add_f32 %2, %6, %7\n\tv_add_f32 %3, %10, %11\n\tv_add_f32 %4, %8, %9\n\tv_add_f32 %5, %12, %13\n\t"
        "v_cvt_pk_bf16_f32 %0, %2, %3\n\tv_cvt_pk_bf16_f32 %1, %4, %5"
        : "=&v"(r0), "=&v"(r1), "=&v"(t0), "=&v"(t1), "=&v"(t2), "=&v"(t3)
        : "v"(a[0]), "v"(a[1]), "v"(a[2]), "v"(a[3]), "v"(b[0]), "v"(b[1]), "v"(b[2]), "v"(b[3]));
}
__device__ __forceinline__ void pack_cols(const f32x4 a, const f32x4 b, unsigned& r0, unsigned& r1) {
    float t0, t1, t2, t3;
    asm("v_add_f32 %2, %6, %7\n\tv_add_f32 %3, %8, %9\n\tv_add_f32 %4, %10, %11\n\tv_add_f32 %5, %12, %13\n\t"
        "v_cvt_pk_bf16_f32 %0, %2, %3\n\tv_cvt_pk_bf16_f32 %1, %4, %5"
        : "=&v"(r0), "=&v"(r1), "=&v"(t0), "=&v"(t1), "=&v"(t2), "=&v"(t3)
        : "v"(a[0]), "v"(a[1]), "v"(a[2]), "v"(a[3]), "v"(b[0]), "v"(b[1]), "v"(b[2]), "v"(b[3]));
}
__device__ __forceinline__ u32x4 scale_pack(const f32x4 a, const f32x4 b, float r) {
    u32x4 o; float t0, t1;
    asm("v_mul_f32 %4, %6, %14\n\tv_mul_f32 %5, %10, %14\n\tv_cvt_pk_bf16_f32 %0, %4, %5\n\t"
        "v_mul_f32 %4, %7, %14\n\tv_mul_f32 %5, %11, %14\n\tv_cvt_pk_bf16_f32 %1, %4, %5\n\t"
        "v_mul_f32 %4, %8, %14\n\tv_mul_f32 %5, %12, %14\n\tv_cvt_pk_bf16_f32 %2, %4, %5\n\t"
        "v_mul_f32 %4, %9, %14\n\tv_mul_f32 %5, %13, %14\n\tv_cvt_pk_bf16_f32 %3, %4, %5"
        : "=&v"(o.x), "=&v"(o.y), "=&v"(o.z), "=&v"(o.w), "=&v"(t0), "=&v"(t1)
        : "v"(a[0]), "v"(a[1]), "v"(a[2]), "v"(a[3]), "v"(b[0]), "v"(b[1]), "v"(b[2]), "v"(b[3]), "v"(r));
    return o;
}
__device__ __forceinline__ u32x4 part_pack(const f32x4 a, const f32x4 b, unsigned lim) {
    u32x4 o;
    asm("v_add_f32 %0, %4, %5\n\tv_add_f32 %1, %8, %9\n\tv_add_f32 %2, %6, %7\n\tv_add_f32 %3, %10, %11\n\t"
        "v_min_u32 %0, %0, %12\n\tv_min_u32 %1, %1, %12\n\tv_min_u32 %2, %2, %12\n\tv_min_u32 %3, %3, %12"
        : "=&v"(o.x), "=&v"(o.y), "=&v"(o.z), "=&v"(o.w)
        : "v"(a[0]), "v"(a[1]), "v"(a[2]), "v"(a[3]), "v"(b[0]), "v"(b[1]), "v"(b[2]), "v"(b[3]), "s"(lim));
    return o;
}
__device__ __forceinline__ float neg_max10(const f32x4 m_, const f32x4 a, const f32x4 b, float sc) {
    float m, t;
    asm("v_max3_f32 %0, %2, %3, %4\n\tv_max3_f32 %1, %5, %6, %7\n\tv_max3_f32 %0, %0, %8, %9\n\tv_max3_f32 %1, %1, %10, %11\n\tv_max_f32 %0, %0, %1\n\t"
        "v_mov_b32 %1, %0\n\ts_nop 1\n\tv_permlane16_swap_b32 %0, %1\n\tv_max_f32 %0, %0, %1\n\t"
        "v_mov_b32 %1, %0\n\ts_nop 1\n\tv_permlane32_swap_b32 %0, %1\n\tv_max_f32 %0, %0, %1\n\tv_mul_f32 %0, %0, %12"
        : "=&v"(m), "=&v"(t)
        : "v"(a[0]), "v"(a[1]), "v"(a[2]), "v"(a[3]), "v"(b[0]), "v"(b[1]), "v"(b[2]), "v"(b[3]), "v"(m_[0]), "v"(m_[1]), "v"(sc));
    return m;
}
__device__ __forceinline__ float recip_sum10(const float (&p0)[4], const float (&p1)[4], float q0, float q1) {
    float a, b, c;
    asm("v_add_f32 %0, %3, %4\n\tv_add_f32 %1, %5, %6\n\tv_add_f32 %2, %7, %8\n\tv_add_f32 %0, %0, %9\n\tv_add_f32 %1, %1, %10\n\tv_add_f32 %2, %2, %11\n\t"
        "v_add_f32 %0, %0, %12\n\tv_add_f32 %1, %1, %2\n\tv_add_f32 %0, %0, %1\n\t"
        "v_mov_b32 %1, %0\n\ts_nop 1\n\tv_permlane16_swap_b32 %0, %1\n\tv_add_f32 %0, %0, %1\n\t"
        "v_mov_b32 %1, %0\n\ts_nop 1\n\tv_permlane32_swap_b32 %0, %1\n\tv_add_f32 %0, %0, %1\n\tv_rcp_f32 %0, %0\n\ts_nop 0"
        : "=&v"(a), "=&v"(b), "=&v"(c)
        : "v"(p0[0]), "v"(p0[1]), "v"(p0[2]), "v"(p0[3]), "v"(p1[0]), "v"(p1[1]), "v"(p1[2]), "v"(p1[3]), "v"(q0), "v"(q1));
    return a;
}
#define MF0(acc, A_, B_) asm volatile("v_mfma_f32_16x16x32_bf16 %0, %1, %2, 0" : "=&v"(acc) : "v"(A_), "v"(B_))
#define MFA(acc, A_, B_) asm volatile("v_mfma_f32_16x16x32_bf16 %0, %1, %2, %0" : "+v"(acc) : "v"(A_), "v"(B_))
struct ChainArgs {
    const bf16* x; bf16* out; const bf16* ax; const bf16* awqkv; const bf16* awo;
    const bf16* mem0; const bf16* Wq; const bf16* Wk; const bf16* Wv; const bf16* Wo;
    unsigned char* ws;
};

__device__ __forceinline__ bool wait_flag(gu32* f, unsigned want, gu32* tmo) {
    for (unsigned spins = 0;; ++spins) {
        const unsigned v = __builtin_amdgcn_readfirstlane(__hip_atomic_load(f, RLX_AGENT));
        if (v == want) return true;
        if (spins > SPIN_CAP) { __hip_atomic_store(tmo, 1u, RLX_AGENT); return false; }
        __builtin_amdgcn_s_sleep(PSLEEP);
    }
}

__device__ __forceinline__ void chain_role(const ChainArgs& a, const int b) {
    const int h = b >> 1, nh = b & 1;
    const int tid = threadIdx.x, lane = tid & 63, w = __builtin_amdgcn_readfirstlane(tid >> 6);
    const int g = lane >> 4, c = lane & 15;
    const __amdgpu_buffer_rsrc_t ws_r = __builtin_amdgcn_make_buffer_rsrc(a.ws, 0, 0x7fffffff, 0x00020000);
    gu32* flags = (gu32*)(a.ws + WS_FLAG);
    gu32* tmo = flags + FL_TMO;

    {
        const u32x4 pz = {~0u, ~0u, ~0u, ~0u};
#pragma unroll
        for (int sl = 0; sl < RING; ++sl)
#pragma unroll
            for (int k = 0; k < 2; ++k) __builtin_amdgcn_raw_buffer_store_b128(pz, ws_r, (unsigned)((k * 256 + tid) * 16), (unsigned)(WS_PART + (size_t)(sl * 64 + b) * 8192), 16);
        asm volatile("s_waitcnt vmcnt(0)" ::: "memory");
        wg_barrier();
        if (tid == 0) __hip_atomic_fetch_add(flags + FL_RDY, 1u, RLX_AGENT);
    }
    u32x8 W[3][2][8];
    u32x4 pend[2 * (8 - NVREG) * 2];
    {
        const int rj[8] = {0, 1, 8, 9, 2, 3, 10, 11};
        const int rj2[8] = {0, 1, 2, 3, 8, 9, 10, 11};
        u32x4 raw[4][8];
        auto ld_raw = [&](const bf16* Wm) {
#pragma unroll
            for (int it = 0; it < 4; ++it) {
                const int rb = 32 * (4 * w + it) + 16 * (g >> 1) + 4 * (g & 1);
#pragma unroll
                for (int j = 0; j < 8; ++j) raw[it][j] = *(const u32x4*)(Wm + (size_t)(rb + rj[j]) * 4096);
            }
        };
        ld_raw(a.Wk + h * 128 + 8 * c);
#pragma unroll
        for (int Mi = 0; Mi < 3; ++Mi) {
            const int M = Mi == 0 ? 1 : (Mi == 1 ? 0 : 2);
#pragma unroll
            for (int it = 0; it < 4; ++it) {
                u32x4 f[8];
                tr8x8(raw[it], f);
#pragma unroll
                for (int t = 0; t < 8; ++t) lds_wr16(L_STG + (unsigned)(((t * 16 + 4 * w + it) * 64 + lane) * 16), f[t]);
            }
            if (Mi == 0) ld_raw(a.Wq + h * 128 + 8 * c);
            if (Mi == 1) ld_raw(a.Wv + h * 128 + 8 * c);
            if (Mi == 2) {
#pragma unroll
                for (int half = 0; half < 2; ++half) {
                    const int rb = 32 * w + 16 * (g & 1) + 4 * (g >> 1);
                    const bf16* Wp = a.Wo + (size_t)h * 128 * 512 + 256 * nh + 8 * (16 * half + c);
#pragma unroll
                    for (int j = 0; j < 8; ++j) raw[half][j] = *(const u32x4*)(Wp + (size_t)(rb + rj2[j]) * 512);
                }
            }
            wg_barrier();
#pragma unroll
            for (int tau = 0; tau < 2; ++tau)
#pragma unroll
                for (int kk = 0; kk < 8; ++kk) {
                    const int kr = (kk + 2 * w) & 7;
                    const u32x4 lo = lds_rd16(L_STG + (unsigned)((((2 * w + tau) * 16 + 2 * kr) * 64 + lane) * 16));
                    const u32x4 hi = lds_rd16(L_STG + (unsigned)((((2 * w + tau) * 16 + 2 * kr + 1) * 64 + lane) * 16));
                    u32x8 v = {lo.x, lo.y, lo.z, lo.w, hi.x, hi.y, hi.z, hi.w};
                    W[M][tau][kk] = v;
                    if (M < 2) asm volatile("" : "+a"(W[M][tau][kk]));
                    else if (kk >= NVREG) {
                        pend[(tau * (8 - NVREG) + kk - NVREG) * 2] = lo; pend[(tau * (8 - NVREG) + kk - NVREG) * 2 + 1] = hi;
                    }
                }
            wg_barrier();
        }
#pragma unroll
        for (int q2 = 0; q2 < 2 * (8 - NVREG) * 2; ++q2) lds_wr16(L_WV + (unsigned)(((w * 2 * (8 - NVREG) * 2 + q2) * 64 + lane) * 16), pend[q2]);
#pragma unroll
        for (int half = 0; half < 2; ++half) {
            const int m = 16 * half + c;
            u32x4 f[8];
            tr8x8(raw[half], f);
#pragma unroll
            for (int t = 0; t < 8; ++t) {
                const int n = 8 * m + t;
                const int ww = n >> 6, n6 = n & 63, G4 = n6 >> 4, n4 = n6 & 15;
                const int tp = 2 * (G4 >> 1) + (n4 & 1), cc = 8 * (G4 & 1) + (n4 >> 1);
                lds_wr16(L_WO + (unsigned)((((ww * 4 + tp) * 4 + w) * 64 + g * 16 + cc) * 16), f[t]);
            }
        }
    }
    u32x4 m0, m1;
    m0 = *(const u32x4*)(a.mem0 + (c >> 1) * 512 + 64 * (2 * w) + 16 * g + 8 * (c & 1));
    m1 = *(const u32x4*)(a.mem0 + (c >> 1) * 512 + 64 * (2 * w + 1) + 16 * g + 8 * (c & 1));
    lds_wr16(L_MEM + (unsigned)((2 * w * 64 + lane) * 16), m0);
    lds_wr16(L_MEM + (unsigned)(((2 * w + 1) * 64 + lane) * 16), m1);
    if (w == 1 || w == 2) {
        wait_flag(flags + FL_KV + (0 * 32 + h) * FS, 1u, tmo);
        const unsigned src = (unsigned)((w == 1 ? WS_KX : WS_VX) + (size_t)(0 * 32 + h) * 8192);
        const unsigned dst = (w == 1 ? L_KX : L_VX);
        u32x4 kv[8];
#pragma unroll
        for (int k = 0; k < 8; ++k) kv[k] = __builtin_amdgcn_raw_buffer_load_b128(ws_r, src + k * 1024 + lane * 16, 0, 16);
#pragma unroll
        for (int k = 0; k < 8; ++k) lds_wr16(dst + k * 1024 + lane * 16, kv[k]);
    }
    if (w == 0) wait_flag(flags + FL_RDY, 63u, tmo);
    if (tid == 0) lds_wr4(L_FLAG, 0);
    wg_barrier();

    const int idx = (c & 1) ? 0xEEEE : 0x4444;
    const float sc2 = 0.08838834764831845f * 1.4426950408889634f;
    unsigned epos = (unsigned)(((c >> 2) * 64 + (2 * (c & 1) + (w >> 1)) * 16 + 2 * g) * 16 + (4 * ((c >> 1) & 1) + 2 * (w & 1)) * 2);
    const int gp = c >> 2, rp = c & 3;
    unsigned qwo = L_Q + epos;
    unsigned l16 = (unsigned)lane * 16;
    unsigned a0o = 8192u + (unsigned)((g * 16 + 2 * gp + (rp & 1)) * 16);
    unsigned a2o = 4096u + (unsigned)((g * 16 + 4 * gp + 2 + (rp & 1)) * 16);
    unsigned qao = L_Q + (unsigned)((g * 16 + (c & 7)) * 16);
    unsigned wvb = L_WV + (unsigned)(w * 2 * (8 - NVREG) * 2 * 1024) + l16;
    unsigned wob = L_WO + (unsigned)(w * 16384) + l16;
    unsigned owo = (c < 8) ? L_O + (unsigned)(((g >> 1) * 64 + (2 * (g & 1) + (w >> 1)) * 16 + 2 * (c & 7) + (w & 1)) * 16) : L_DUMMY + (unsigned)tid * 16;
    unsigned pvo = (unsigned)((((2 * (16 * nh + 4 * w + (c >> 3)) + (g >> 1)) * 32 + h) * 2 + (g & 1)) * 128 + (c & 7) * 16);

    asm volatile("" : "+v"(l16), "+v"(a0o), "+v"(a2o), "+v"(qao), "+v"(wvb), "+v"(wob), "+v"(owo), "+v"(pvo), "+v"(epos), "+v"(qwo));
    f32x4 aq[2], ak[2], av[2];
    u32x4 pm0 = {0u, 0u, 0u, 0u}, pm1 = pm0, vmt = pm0;
#define ZACC() do { _Pragma("unroll") for (int t = 0; t < 2; ++t) { aq[t] = (f32x4){0, 0, 0, 0}; ak[t] = aq[t]; av[t] = aq[t]; } \
        asm volatile("" : "+v"(aq[0]), "+v"(aq[1]), "+v"(ak[0]), "+v"(ak[1]), "+v"(av[0]), "+v"(av[1])); } while (0)
    ZACC();
#define EARLY() do { asm volatile("s_nop 1" : "+v"(m0), "+v"(m1), "+v"(ak[0]), "+v"(ak[1]), "+v"(aq[0]), "+v"(aq[1])); \
        SMF_A(ak[0], m0, W[1][0][0]); SMF_A(ak[1], m0, W[1][1][0]); SMF_A(aq[0], m0, W[0][0][0]); SMF_A(aq[1], m0, W[0][1][0]); \
        SMF_A(ak[0], m1, W[1][0][1]); SMF_A(ak[1], m1, W[1][1][1]); SMF_A(aq[0], m1, W[0][0][1]); SMF_A(aq[1], m1, W[0][1][1]); } while (0)
    EARLY();
    unsigned amo[6];
#pragma unroll
    for (int j = 0; j < 6; ++j) { amo[j] = L_MEM + l16 + (unsigned)(((j + 2 + 2 * w) & 7) * 1024); asm volatile("" : "+v"(amo[j])); }
    for (int s = 0; s < 64; ++s) {
        const unsigned kxb = L_KX + (unsigned)(s & 1) * 12288, vxb = L_VX + (unsigned)(s & 1) * 8192;
        unsigned kvflag = (unsigned)(s + 2);
        if ((w == 1 || w == 2) && s < 63) kvflag = __hip_atomic_load(flags + FL_KV + (((s + 1) % KVR) * 32 + h) * FS, RLX_AGENT);
#define SB() __builtin_amdgcn_sched_barrier(0)
        u32x4 A[8];
        const unsigned kxl = kxb + l16, k0l = kxb + a0o, vxl = vxb + (unsigned)(2 * w * 1024) + l16, kml = kxb + 8192 + epos;
        u32x4 ka[4], kb[4], vx[2], qf[4], k0[4], wl[8], wq[16];
#define RDA(k) A[k] = lds_rd16(amo[(k) - 2])
#define RDWL(i) wl[i] = lds_rd16(wvb + (unsigned)((i) * 1024))
#define RDWO(i) wq[i] = lds_rd16(wob + (unsigned)((i) * 1024))
#define CAT8(lo, hi) ((u32x8){lo.x, lo.y, lo.z, lo.w, hi.x, hi.y, hi.z, hi.w})
        A[0] = m0; A[1] = m1;
        RDA(2); RDA(3); RDA(4);
        SB();
        SMF_A(ak[0], A[2], W[1][0][2]); RDA(5); SB(); SMF_A(ak[1], A[2], W[1][1][2]); RDA(6); SB();
        SMF_A(ak[0], A[3], W[1][0][3]); RDA(7); SB(); SMF_A(ak[1], A[3], W[1][1][3]); RDWL(0); SB();
        SMF_A(ak[0], A[4], W[1][0][4]); RDWL(1); SB(); SMF_A(ak[1], A[4], W[1][1][4]); RDWL(2); SB();
        SMF_A(ak[0], A[5], W[1][0][5]); RDWL(3); SB(); SMF_A(ak[1], A[5], W[1][1][5]); RDWL(4); SB();
        SMF_A(ak[0], A[6], W[1][0][6]); RDWL(5); SB(); SMF_A(ak[1], A[6], W[1][1][6]); RDWL(6); SB();
        SMF_A(ak[0], A[7], W[1][0][7]); RDWL(7); SB(); SMF_A(ak[1], A[7], W[1][1][7]);
#pragma unroll
        for (int kk = 2; kk < 5; ++kk) { SMF_A(aq[0], A[kk], W[0][0][kk]); SMF_A(aq[1], A[kk], W[0][1][kk]); }
        asm volatile("s_nop 1" : "+v"(ak[0]), "+v"(ak[1]));
        { unsigned r0, r1; pack_rows(ak[0], ak[1], r0, r1); lds_wr4(kml, r0); lds_wr4(kml + 16, r1); }
        SB();
#pragma unroll
        for (int kk = 5; kk < 8; ++kk) { SMF_A(aq[0], A[kk], W[0][0][kk]); SMF_A(aq[1], A[kk], W[0][1][kk]); }
        { const u32x8 w0 = CAT8(wl[0], wl[1]), w1 = CAT8(wl[2], wl[3]), w2 = CAT8(wl[4], wl[5]), w3 = CAT8(wl[6], wl[7]);
          SMF_V(av[0], A[6], w0); SMF_V(av[1], A[6], w2); SMF_V(av[0], A[7], w1); SMF_V(av[1], A[7], w3); }
        asm volatile("s_nop 3" : "+v"(aq[0]), "+v"(aq[1]));
        { unsigned r0, r1; pack_rows(aq[0], aq[1], r0, r1); lds_wr4(qwo, r0); lds_wr4(qwo + 16, r1); }
        SB();
        SMF_V(av[0], A[0], W[2][0][0]); ka[0] = lds_rd16(kxl); SB(); SMF_V(av[1], A[0], W[2][1][0]); kb[0] = lds_rd16(kxl + 4096); SB();
        SMF_V(av[0], A[1], W[2][0][1]); ka[1] = lds_rd16(kxl + 1024); SB(); SMF_V(av[1], A[1], W[2][1][1]); SB();
        asm volatile("s_waitcnt lgkmcnt(3)" ::: "memory");
        __builtin_amdgcn_s_barrier(); asm volatile("" ::: "memory");
        qf[0] = lds_rd16(qao); k0[0] = lds_rd16(k0l); SB();
        SMF_V(av[0], A[2], W[2][0][2]); qf[1] = lds_rd16(qao + 1024); SB(); SMF_V(av[1], A[2], W[2][1][2]); k0[1] = lds_rd16(k0l + 1024); SB();
        SMF_V(av[0], A[3], W[2][0][3]); kb[1] = lds_rd16(kxl + 4096 + 1024); SB(); SMF_V(av[1], A[3], W[2][1][3]); qf[2] = lds_rd16(qao + 2048); SB();
        f32x4 Sa, Sb, Sm;
        MF0(Sm, k0[0], qf[0]); k0[2] = lds_rd16(k0l + 2048); SB();
        MF0(Sa, ka[0], qf[0]); ka[2] = lds_rd16(kxl + 2048); SB();
        MF0(Sb, kb[0], qf[0]); kb[2] = lds_rd16(kxl + 4096 + 2048); SB();
        MFA(Sm, k0[1], qf[1]); qf[3] = lds_rd16(qao + 3072); SB();
        MFA(Sa, ka[1], qf[1]); k0[3] = lds_rd16(k0l + 3072); SB();
        MFA(Sb, kb[1], qf[1]); ka[3] = lds_rd16(kxl + 3072); SB();
        MFA(Sm, k0[2], qf[2]); kb[3] = lds_rd16(kxl + 4096 + 3072); SB();
        MFA(Sa, ka[2], qf[2]); vx[0] = lds_rd16(vxl); SB();
        MFA(Sb, kb[2], qf[2]); vx[1] = lds_rd16(vxl + 1024); SB();
        MFA(Sm, k0[3], qf[3]); RDWO(0); SB();
        MFA(Sa, ka[3], qf[3]); RDWO(1); SB();
        MFA(Sb, kb[3], qf[3]); RDWO(2); SB();
        SMF_V(av[0], A[4], W[2][0][4]); RDWO(3); SB(); SMF_V(av[1], A[4], W[2][1][4]); RDWO(4); SB();
        SMF_V(av[0], A[5], W[2][0][5]); RDWO(5); SB(); SMF_V(av[1], A[5], W[2][1][5]); RDWO(6); SB();
        asm volatile("" : "+v"(Sm), "+v"(Sa), "+v"(Sb));
        const float nmb = neg_max10(Sm, Sa, Sb, -sc2);
        float pa[4], pb[4];
        RDWO(7);
        pa[0] = __builtin_amdgcn_exp2f(__builtin_fmaf(Sa[0], sc2, nmb)); pa[1] = __builtin_amdgcn_exp2f(__builtin_fmaf(Sa[1], sc2, nmb)); RDWO(8); SB();
        pa[2] = __builtin_amdgcn_exp2f(__builtin_fmaf(Sa[2], sc2, nmb)); pa[3] = __builtin_amdgcn_exp2f(__builtin_fmaf(Sa[3], sc2, nmb)); RDWO(9); SB();
        pb[0] = __builtin_amdgcn_exp2f(__builtin_fmaf(Sb[0], sc2, nmb)); pb[1] = __builtin_amdgcn_exp2f(__builtin_fmaf(Sb[1], sc2, nmb)); RDWO(10); SB();
        pb[2] = __builtin_amdgcn_exp2f(__builtin_fmaf(Sb[2], sc2, nmb)); pb[3] = __builtin_amdgcn_exp2f(__builtin_fmaf(Sb[3], sc2, nmb)); RDWO(11); SB();
        const float pm_0 = __builtin_amdgcn_exp2f(__builtin_fmaf(Sm[0], sc2, nmb)), pm_1 = __builtin_amdgcn_exp2f(__builtin_fmaf(Sm[1], sc2, nmb));
        RDWO(12);
        u32x4 px = {pk2(pa[0], pa[1]), pk2(pa[2], pa[3]), pk2(pb[0], pb[1]), pk2(pb[2], pb[3])};
        { const unsigned pmp = pk2(pm_0, pm_1); pm0.x = pmp; pm1.y = pmp; }
        asm volatile("" : "+v"(av[0]), "+v"(av[1]));
        { unsigned r0, r1; pack_cols(av[0], av[1], r0, r1); vmt.x = r0; vmt.y = r1; }
        f32x4 o[2];
        asm volatile("s_nop 1" : "+v"(px), "+v"(pm0), "+v"(pm1), "+v"(vmt));
        MF0(o[0], vx[0], px); RDWO(13); SB(); MF0(o[1], vx[1], px); RDWO(14); SB();
        MFA(o[0], vmt, pm0); RDWO(15); SB(); MFA(o[1], vmt, pm1);
        lds_wr8(L_VM + (unsigned)(w * 512) + (l16 >> 1), (u32x2){vmt.x, vmt.y});
        SB();
        const float rl = recip_sum10(pa, pb, pm_0, pm_1);
        SB();
        asm volatile("" : "+v"(o[0]), "+v"(o[1]));
        lds_wr16(owo, scale_pack(o[0], o[1], rl));
        f32x4 pp[4];
#pragma unroll
        for (int tp = 0; tp < 4; ++tp) pp[tp] = (f32x4){0, 0, 0, 0};
        wg_barrier();
        const u32x4 oa0 = lds_rd16(L_O + l16);
        const u32x4 oa1 = lds_rd16(L_O + 1024 + l16);
        asm volatile("s_nop 1" : "+v"(pp[0]), "+v"(pp[1]), "+v"(pp[2]), "+v"(pp[3]));
        {
          const u32x8 w00 = CAT8(wq[0], wq[1]), w01 = CAT8(wq[2], wq[3]), w10 = CAT8(wq[4], wq[5]), w11 = CAT8(wq[6], wq[7]);
          const u32x8 w20 = CAT8(wq[8], wq[9]), w21 = CAT8(wq[10], wq[11]), w30 = CAT8(wq[12], wq[13]), w31 = CAT8(wq[14], wq[15]);
          SMF_V(pp[0], oa0, w00); SMF_V(pp[1], oa0, w10); SMF_V(pp[2], oa0, w20); SMF_V(pp[3], oa0, w30);
          SMF_V(pp[0], oa1, w01); SMF_V(pp[1], oa1, w11); SMF_V(pp[2], oa1, w21); SMF_V(pp[3], oa1, w31); }
        asm volatile("s_nop 5" : "+v"(pp[0]), "+v"(pp[1]));
        u32x4 pv[2];
#pragma unroll
        for (int u = 0; u < 2; ++u) {
            if (u == 1) asm volatile("" : "+v"(pp[2]), "+v"(pp[3]));
            const u32x4 v = part_pack(pp[2 * u], pp[2 * u + 1], 0xFFFFFFFEu);
            pv[u] = v;
            __builtin_amdgcn_raw_buffer_store_b128(v, ws_r, pvo + (unsigned)(u * 4 * 8192), (unsigned)(WS_PART + (size_t)(s % RING) * 64 * 8192), 16);
        }
        SB();
        int ln = lane; asm volatile("" : "+v"(ln));
        if (w == 3) {
            if (nh == 0) {
#pragma unroll
                for (int q2 = 0; q2 < 2; ++q2) {
                    const int ch = q2 * 64 + ln;
                    const u32x4 v = lds_rd16(kxb + 8192 + (unsigned)((((ch >> 5) * 64) + ((ch >> 3) & 3) * 16 + (ch & 7)) * 16));
                    __builtin_amdgcn_raw_buffer_store_b128(v, ws_r, (unsigned)(WS_KM + (size_t)((s % KVR) * 32 + h) * 2048) + ch * 16, 0, 16);
                }
            } else {
#pragma unroll
                for (int q2 = 0; q2 < 2; ++q2) {
                    const u32x4 v = lds_rd16(L_VM + (unsigned)((q2 * 64 + ln) * 16));
                    __builtin_amdgcn_raw_buffer_store_b128(v, ws_r, (unsigned)(WS_VM + (size_t)((s % KVR) * 32 + h) * 2048) + (q2 * 64 + ln) * 16, 0, 16);
                }
            }
            asm volatile("s_waitcnt vmcnt(0)" ::: "memory");
            if (ln == 0) __hip_atomic_store(flags + (nh == 0 ? FL_KM : FL_VM) + ((s % KVR) * 32 + h) * FS, (unsigned)(s + 1), RLX_AGENT);
        }
        if (s == 63) break;
        if (w == 1 || w == 2) {
                        if (__builtin_amdgcn_readfirstlane(kvflag) != (unsigned)(s + 2)) wait_flag(flags + FL_KV + (((s + 1) % KVR) * 32 + h) * FS, (unsigned)(s + 2), tmo);
            const unsigned src = (unsigned)((w == 1 ? WS_KX : WS_VX) + (size_t)(((s + 1) % KVR) * 32 + h) * 8192);
            const unsigned dst = (w == 1 ? L_KX + (unsigned)((s + 1) & 1) * 12288 : L_VX + (unsigned)((s + 1) & 1) * 8192);
            u32x4 kv[8];
#pragma unroll
            for (int k = 0; k < 8; ++k) kv[k] = __builtin_amdgcn_raw_buffer_load_b128(ws_r, src + k * 1024 + ln * 16, 0, 16);
#pragma unroll
            for (int k = 0; k < 8; ++k) lds_wr16(dst + k * 1024 + ln * 16, kv[k]);
        }
        const unsigned pbase = (unsigned)(WS_PART + (size_t)(s % RING) * 64 * 8192) + (unsigned)b * 8192;
        if (w == 0) {
            for (int d = 0; d < DELAY_A; ++d) __builtin_amdgcn_s_sleep(1);
            const unsigned loff = (unsigned)((((2 * (ln >> 5) + ((ln >> 3) & 1)) * 2 + ((ln >> 4) & 1)) * 8 + (ln & 7)) * 16);
            const unsigned mdst = (unsigned)(WS_MEMS + (size_t)((s + 1) % MR) * 8192) + (unsigned)(b * 128 + (ln & 31) * 4);
            u32x4 X[8];
            auto ldp = [&](u32x4 (&p)[8], unsigned rep) {
#pragma unroll
                for (int k = 0; k < 8; ++k) p[k] = __builtin_amdgcn_raw_buffer_load_b128(ws_r, loff + k * 1024, pbase + rep * 64 * 8192, 16);
                asm volatile("" ::: "memory");
            };
            auto tryred = [&](u32x4 (&p)[8]) -> bool {
                f32x2 a0 = {__uint_as_float(p[0].x), __uint_as_float(p[0].y)}, a1 = {__uint_as_float(p[0].z), __uint_as_float(p[0].w)};
                f32x2 b0 = {__uint_as_float(p[1].x), __uint_as_float(p[1].y)}, b1 = {__uint_as_float(p[1].z), __uint_as_float(p[1].w)};
#pragma unroll
                for (int k = 2; k < 8; k += 2) {
                    a0 += (f32x2){__uint_as_float(p[k].x), __uint_as_float(p[k].y)}; a1 += (f32x2){__uint_as_float(p[k].z), __uint_as_float(p[k].w)};
                    b0 += (f32x2){__uint_as_float(p[k + 1].x), __uint_as_float(p[k + 1].y)}; b1 += (f32x2){__uint_as_float(p[k + 1].z), __uint_as_float(p[k + 1].w)};
                }
                a0 += b0; a1 += b1;
                float f0 = a0.x, f1 = a0.y, f2 = a1.x, f3 = a1.y;
                f0 += xor8f(f0); f1 += xor8f(f1); f2 += xor8f(f2); f3 += xor8f(f3);
                { float t0 = f0, t1 = f1, t2 = f2, t3 = f3; swap32(f0, t0); swap32(f1, t1); swap32(f2, t2); swap32(f3, t3); f0 += t0; f1 += t1; f2 += t2; f3 += t3; }
                const float chk = (f0 + f1) + (f2 + f3);
                if (__any(chk != chk)) {
                    unsigned mx = 0;
#pragma unroll
                    for (int k = 0; k < 8; ++k) mx = max(mx, max4(p[k]));
                    if (!__all(mx != ~0u)) return false;
                }
                const unsigned d0 = pk2(f0, f1), d1 = pk2(f2, f3);
                const unsigned dv = min((ln & 8) ? d1 : d0, 0xFFFEFFFFu);
                if (ln < 32) __builtin_amdgcn_raw_buffer_store_b32(dv, ws_r, mdst, 0, 16);
                if (ln == 0) lds_wr4(L_FLAG, (unsigned)(s + 1));
                return true;
            };
            for (unsigned spins = 0;; ++spins) {
                ldp(X, 0);
                if (tryred(X)) break;
                if (spins > SPIN_CAP) { __hip_atomic_store(tmo, 2u, RLX_AGENT); if (ln == 0) lds_wr4(L_FLAG, (unsigned)(s + 1)); break; }
            }
        } else {
            for (unsigned spins = 0; lds_rd4(L_FLAG) != (unsigned)(s + 1); ++spins) { asm volatile("" ::: "memory"); __builtin_amdgcn_s_sleep(1); if (spins > SPIN_CAP) break; }
            if (w == 3) {
                const u32x4 pz = {~0u, ~0u, ~0u, ~0u};
#pragma unroll
                for (int k = 0; k < 8; ++k) __builtin_amdgcn_raw_buffer_store_b128(pz, ws_r, (unsigned)(k * 1024 + ln * 16), pbase, 16);
            }
        }
        ZACC();
        {
            for (int d = 0; d < DELAY_B; ++d) __builtin_amdgcn_s_sleep(1);
            const unsigned mo = (unsigned)(WS_MEMS + (size_t)((s + 1) % MR) * 8192) + (unsigned)(w * 2048 + ln * 16);
            for (unsigned spins = 0;; ++spins) {
                asm volatile("" ::: "memory");
                m0 = __builtin_amdgcn_raw_buffer_load_b128(ws_r, mo, 0, 16);
                m1 = __builtin_amdgcn_raw_buffer_load_b128(ws_r, mo + 1024, 0, 16);
                if (__all(max(max4(m0), max4(m1)) != ~0u)) break;
                if (spins > SPIN_CAP) { __hip_atomic_store(tmo, 3u, RLX_AGENT); break; }
            }
            lds_wr16(L_MEM + (unsigned)(w * 2048 + ln * 16), m0);
            lds_wr16(L_MEM + (unsigned)(w * 2048 + 1024 + ln * 16), m1);
            EARLY();
            wg_barrier();
        }
    }
}


template <int NATURAL, int PIN, int KMODE = 0, int LOWREG = 0>
__device__ __forceinline__ void load_panel(const bf16* Wp, const int ld, u32x4 (&F)[2][16], const int w, const int lane, const unsigned base = 0) {
    const int g = lane >> 4, c = lane & 15;
    u32x4 r[4][8];
    auto ldr = [&](int it) {
        const int kk = 8 * (it >> 1) + 2 * w + (it & 1);
#pragma unroll
        for (int j = 0; j < 8; ++j) r[it][j] = *(const u32x4*)(Wp + (size_t)(KMODE ? (32 * kk + 16 * (j >> 2) + 4 * g + (j & 3)) : (32 * kk + 8 * g + j)) * ld + 8 * c);
    };
    ldr(0); ldr(1);
    if (!LOWREG) { ldr(2); ldr(3); }
#pragma unroll
    for (int hf = 0; hf < 2; ++hf) {
#pragma unroll
        for (int i2 = 0; i2 < 2; ++i2) {
            u32x4 f[8];
            tr8x8(r[2 * hf + i2], f);
#pragma unroll
            for (int t = 0; t < 8; ++t) lds_wr16(base + (unsigned)((t * 514 + (2 * w + i2) * 64 + lane) * 16), f[t]);
        }
        if (LOWREG && hf == 0) { ldr(2); ldr(3); }
        wg_barrier();
#pragma unroll
        for (int tau = 0; tau < 2; ++tau)
#pragma unroll
            for (int k8 = 0; k8 < 8; ++k8) {
                const int T = 2 * w + tau;
                const unsigned ad = base + (NATURAL ? (unsigned)(((c & 7) * 514 + k8 * 64 + g * 16 + 2 * T + (c >> 3)) * 16) : (unsigned)((T * 514 + k8 * 64 + lane) * 16));
                F[tau][8 * hf + k8] = lds_rd16(ad);
                if (PIN) asm volatile("" : "+a"(F[tau][8 * hf + k8]));
            }
        wg_barrier();
    }
}
#define MFD(acc, A_, B_, CA, CB) asm volatile("v_mfma_f32_16x16x32_bf16 %0, %1, %2, %0" : "+v"(acc) : CA(A_), CB(B_))

constexpr unsigned H_XSZ = 32 * 1040;
constexpr unsigned H_X = 0;
constexpr unsigned H_X0 = 0;
constexpr unsigned H_ST0 = H_XSZ;
constexpr unsigned H_KM = H_ST0 + 24576;
constexpr unsigned H_VM = H_KM + 4096;
constexpr unsigned H_X1 = H_VM + 2048;
constexpr unsigned H_ST1 = H_X1 + H_XSZ;
constexpr unsigned H_STG2 = H_X1;
__device__ __forceinline__ unsigned h_x(int buf) { return buf ? H_X1 : H_X0; }
__device__ __forceinline__ unsigned h_st(int i) { return i == 0 ? H_ST0 : H_ST1 + (unsigned)(i - 1) * 24576; }

__device__ __forceinline__ void head_role(const ChainArgs& a, const int u) {
    const int h = u >> 1, par = u & 1;
    const int tid = threadIdx.x, lane = tid & 63, w = __builtin_amdgcn_readfirstlane(tid >> 6);
    const int g = lane >> 4, c = lane & 15;
    const __amdgpu_buffer_rsrc_t ws_r = __builtin_amdgcn_make_buffer_rsrc(a.ws, 0, 0x7fffffff, 0x00020000);
    const __amdgpu_buffer_rsrc_t x_r = __builtin_amdgcn_make_buffer_rsrc((void*)a.x, 0, 2048 * 512 * 2, 0x00020000);
    gu32* flags = (gu32*)(a.ws + WS_FLAG);
    gu32* tmo = flags + FL_TMO;
    u32x4 FK[2][16], FV[2][16], FQ[2][16];
    load_panel<1, 1>(a.Wk + h * 128, 4096, FK, w, lane);
    load_panel<0, 0>(a.Wv + h * 128, 4096, FV, w, lane);
    const float sc2 = 0.08838834764831845f * 1.4426950408889634f;
    auto load_x = [&](int s, int buf) {
#pragma unroll
        for (int r = 0; r < 8; ++r) {
            unsigned keep;
            const unsigned voff = (unsigned)(((s * 32 + 8 * w + r) * 512) * 2 + lane * 16), dst = h_x(buf) + (unsigned)(8 * w + r) * 1040;
            asm volatile("s_mov_b32 %0, m0\n\ts_mov_b32 m0, %3\n\ts_nop 0\n\tbuffer_load_dwordx4 %1, %2, 0 offen lds\n\ts_mov_b32 m0, %0" : "=&s"(keep) : "v"(voff), "s"(x_r), "s"(dst) : "memory");
        }
    };
    auto project = [&](const int i, auto dokv_, auto doq_) {
        constexpr bool DOKV = decltype(dokv_)::value, DOQ = decltype(doq_)::value;
        const int s = 2 * i + par;
        const unsigned xb = h_x(i & 1) + (unsigned)(c * 1040 + g * 16);
        f32x4 dk[2][2], dq[2][2], dv[2][2];
#pragma unroll
        for (int t = 0; t < 2; ++t)
#pragma unroll
            for (int tt = 0; tt < 2; ++tt) { dk[t][tt] = (f32x4){0, 0, 0, 0}; dq[t][tt] = dk[t][tt]; dv[t][tt] = dk[t][tt]; }
        asm volatile("s_nop 1" : "+v"(dk[0][0]), "+v"(dk[0][1]), "+v"(dk[1][0]), "+v"(dk[1][1]), "+v"(dq[0][0]), "+v"(dq[0][1]), "+v"(dq[1][0]), "+v"(dq[1][1]));
        asm volatile("s_nop 1" : "+v"(dv[0][0]), "+v"(dv[0][1]), "+v"(dv[1][0]), "+v"(dv[1][1]));
        u32x4 xq[16][2];
#pragma unroll
        for (int kk = 0; kk < 2; ++kk) { xq[kk][0] = lds_rd16(xb + (unsigned)(kk * 64)); xq[kk][1] = lds_rd16(xb + (unsigned)(16 * 1040 + kk * 64)); }
#pragma unroll
        for (int kk = 0; kk < 16; ++kk) {
            if (kk + 2 < 16) { xq[kk + 2][0] = lds_rd16(xb + (unsigned)((kk + 2) * 64)); xq[kk + 2][1] = lds_rd16(xb + (unsigned)(16 * 1040 + (kk + 2) * 64)); }
            __builtin_amdgcn_sched_barrier(0);
            const u32x4 x0 = xq[kk][0], x1 = xq[kk][1];
#pragma unroll
            for (int t = 0; t < 2; ++t) {
                if constexpr (DOKV) {
                    MFD(dk[t][0], FK[t][kk], x0, "a", "v"); MFD(dk[t][1], FK[t][kk], x1, "a", "v");
                    MFD(dv[t][0], x0, FV[t][kk], "v", "v"); MFD(dv[t][1], x1, FV[t][kk], "v", "v");
                }
                if constexpr (DOQ) { MFD(dq[t][0], FQ[t][kk], x0, "a", "v"); MFD(dq[t][1], FQ[t][kk], x1, "a", "v"); }
            }
        }
        asm volatile("s_nop 7" : "+v"(dk[0][0]), "+v"(dk[0][1]), "+v"(dk[1][0]), "+v"(dk[1][1]), "+v"(dq[0][0]), "+v"(dq[0][1]), "+v"(dq[1][0]), "+v"(dq[1][1]));
        asm volatile("" : "+v"(dv[0][0]), "+v"(dv[0][1]), "+v"(dv[1][0]), "+v"(dv[1][1]));
        const unsigned st = h_st(i % 3);
        const unsigned gk = (unsigned)(WS_KX + (size_t)((s % KVR) * 32 + h) * 8192), gv = (unsigned)(WS_VX + (size_t)((s % KVR) * 32 + h) * 8192);
#pragma unroll
        for (int tt = 0; tt < 2; ++tt) {
            const unsigned o = (unsigned)((tt * 4 + w) * 1024 + lane * 16);
            if constexpr (DOKV) {
                const u32x4 kf = {pk2(dk[0][tt][0], dk[0][tt][1]), pk2(dk[0][tt][2], dk[0][tt][3]), pk2(dk[1][tt][0], dk[1][tt][1]), pk2(dk[1][tt][2], dk[1][tt][3])};
                __builtin_amdgcn_raw_buffer_store_b128(kf, ws_r, gk + o, 0, 16);
                lds_wr16(st + 8192 + o, kf);
            }
            if constexpr (DOQ) {
                const u32x4 qf = {pk2(dq[0][tt][0], dq[0][tt][1]), pk2(dq[0][tt][2], dq[0][tt][3]), pk2(dq[1][tt][0], dq[1][tt][1]), pk2(dq[1][tt][2], dq[1][tt][3])};
                lds_wr16(st + o, qf);
            }
        }
        if constexpr (DOKV) {
#pragma unroll
            for (int t = 0; t < 2; ++t) {
                const u32x4 vf = {pk2(dv[t][0][0], dv[t][0][1]), pk2(dv[t][0][2], dv[t][0][3]), pk2(dv[t][1][0], dv[t][1][1]), pk2(dv[t][1][2], dv[t][1][3])};
                const unsigned o = (unsigned)((2 * w + t) * 1024 + lane * 16);
                __builtin_amdgcn_raw_buffer_store_b128(vf, ws_r, gv + o, 0, 16);
                lds_wr16(st + 16384 + o, vf);
            }
            asm volatile("s_waitcnt vmcnt(0)" ::: "memory");
            wg_barrier();
            if (tid == 0) __hip_atomic_store(flags + FL_KV + ((s % KVR) * 32 + h) * FS, (unsigned)(s + 1), RLX_AGENT);
        }
    };
    typedef std::integral_constant<bool, true> T_; typedef std::integral_constant<bool, false> F_;
    load_x(par, 0);
    asm volatile("s_waitcnt vmcnt(0)" ::: "memory");
    wg_barrier();
    project(0, T_{}, F_{});
    load_panel<1, 1, 0, 1>(a.Wq + h * 128, 4096, FQ, w, lane, H_STG2);
    load_x(par + 2, 1);
    project(0, F_{}, T_{});
    for (int i = 1; i < 32 + 2; ++i) {
        if (i < 32) {
            asm volatile("s_waitcnt vmcnt(0)" ::: "memory");
            wg_barrier();
            if (i + 1 < 32) load_x(2 * (i + 1) + par, (i + 1) & 1);
            project(i, T_{}, T_{});
        }
        if (i >= 2) {
            const int s = 2 * (i - 2) + par;
            const unsigned st = h_st((i - 2) % 3);
            if (w == 0) {
                wait_flag(flags + FL_KM + ((s % KVR) * 32 + h) * FS, (unsigned)(s + 1), tmo);
#pragma unroll
                for (int q2 = 0; q2 < 2; ++q2) {
                    const int ch = q2 * 64 + lane;
                    const u32x4 v = __builtin_amdgcn_raw_buffer_load_b128(ws_r, (unsigned)(WS_KM + (size_t)((s % KVR) * 32 + h) * 2048) + ch * 16, 0, 16);
                    lds_wr16(H_KM + (unsigned)((((ch >> 5) * 64) + ((ch >> 3) & 3) * 16 + (ch & 7)) * 16), v);
                }
            } else if (w == 1) {
                wait_flag(flags + FL_VM + ((s % KVR) * 32 + h) * FS, (unsigned)(s + 1), tmo);
#pragma unroll
                for (int q2 = 0; q2 < 2; ++q2) {
                    const u32x4 v = __builtin_amdgcn_raw_buffer_load_b128(ws_r, (unsigned)(WS_VM + (size_t)((s % KVR) * 32 + h) * 2048) + (q2 * 64 + lane) * 16, 0, 16);
                    lds_wr16(H_VM + (unsigned)((q2 * 64 + lane) * 16), v);
                }
            }
            wg_barrier();
            const int qt = w & 1, eh = w >> 1;
            f32x4 Sm, Sa, Sb;
            const unsigned km = H_KM + (unsigned)((g * 16 + 2 * (c >> 2) + (c & 1)) * 16);
            {
                u32x4 q8[4], kmf[4], kaf[4], kbf[4];
#pragma unroll
                for (int ks = 0; ks < 4; ++ks) {
                    q8[ks] = lds_rd16(st + (unsigned)((qt * 4 + ks) * 1024 + lane * 16));
                    kmf[ks] = lds_rd16(km + ks * 1024);
                    kaf[ks] = lds_rd16(st + 8192 + (unsigned)(ks * 1024 + lane * 16));
                    kbf[ks] = lds_rd16(st + 8192 + (unsigned)((4 + ks) * 1024 + lane * 16));
                }
                MF0(Sm, kmf[0], q8[0]); MF0(Sa, kaf[0], q8[0]); MF0(Sb, kbf[0], q8[0]);
#pragma unroll
                for (int ks = 1; ks < 4; ++ks) { MFA(Sm, kmf[ks], q8[ks]); MFA(Sa, kaf[ks], q8[ks]); MFA(Sb, kbf[ks], q8[ks]); }
                asm volatile("s_nop 7" : "+v"(Sm), "+v"(Sa), "+v"(Sb));
            }
            const float NEG = -3.0e38f;
            float sa[4], sb[4];
#pragma unroll
            for (int r = 0; r < 4; ++r) {
                const bool diag = (4 * g + r) <= c;
                sa[r] = (qt == 1 || diag) ? Sa[r] : NEG;
                sb[r] = (qt == 1 && diag) ? Sb[r] : NEG;
            }
            float m = fmaxf(fmaxf(fmaxf(sa[0], sa[1]), fmaxf(sa[2], sa[3])), fmaxf(fmaxf(sb[0], sb[1]), fmaxf(sb[2], sb[3])));
            m = fmaxf(m, fmaxf(Sm[0], Sm[1]));
            m = fmaxf(m, xor16f(m)); m = fmaxf(m, xor32f(m));
            const float nm = -m * sc2;
            float pa[4], pb[4];
#pragma unroll
            for (int r = 0; r < 4; ++r) {
                pa[r] = sa[r] == NEG ? 0.f : __builtin_amdgcn_exp2f(__builtin_fmaf(sa[r], sc2, nm));
                pb[r] = sb[r] == NEG ? 0.f : __builtin_amdgcn_exp2f(__builtin_fmaf(sb[r], sc2, nm));
            }
            const float pm0 = __builtin_amdgcn_exp2f(__builtin_fmaf(Sm[0], sc2, nm)), pm1 = __builtin_amdgcn_exp2f(__builtin_fmaf(Sm[1], sc2, nm));
            float l = ((pa[0] + pa[1]) + (pa[2] + pa[3])) + ((pb[0] + pb[1]) + (pb[2] + pb[3])) + (pm0 + pm1);
            l += xor16f(l); l += xor32f(l);
            const float rl = __builtin_amdgcn_rcpf(l);
            u32x4 px = {pk2(pa[0] * rl, pa[1] * rl), pk2(pa[2] * rl, pa[3] * rl), pk2(pb[0] * rl, pb[1] * rl), pk2(pb[2] * rl, pb[3] * rl)};
            u32x4 pmv = {pk2(pm0 * rl, pm1 * rl), 0u, 0u, 0u};
            f32x4 o[4];
#pragma unroll
            for (int tq = 0; tq < 4; ++tq) {
                const int t = 4 * eh + tq;
                const u32x4 vx = lds_rd16(st + 16384 + (unsigned)(t * 1024 + lane * 16));
                const unsigned vm = lds_rd4(H_VM + (unsigned)(((t >> 1) * 64 + lane) * 8 + (t & 1) * 4));
                u32x4 va = {vm, 0u, 0u, 0u};
                asm volatile("s_nop 1" : "+v"(va), "+v"(px), "+v"(pmv));
                MF0(o[tq], vx, px);
                MFA(o[tq], va, pmv);
            }
            asm volatile("s_nop 7" : "+v"(o[0]), "+v"(o[1]), "+v"(o[2]), "+v"(o[3]));
            const unsigned go = (unsigned)(WS_OX + (size_t)((s * 2 + qt) * 128 + h * 4 + eh * 2) * 1024) + (unsigned)lane * 16;
#pragma unroll
            for (int f = 0; f < 2; ++f) {
                const u32x4 v = {pk2(o[0][2 * f], o[1][2 * f]), pk2(o[2][2 * f], o[3][2 * f]), pk2(o[0][2 * f + 1], o[1][2 * f + 1]), pk2(o[2][2 * f + 1], o[3][2 * f + 1])};
                __builtin_amdgcn_raw_buffer_store_b128(v, ws_r, go + f * 1024, 0, 16);
            }
            asm volatile("s_waitcnt vmcnt(0)" ::: "memory");
            wg_barrier();
            if (tid == 0) __hip_atomic_fetch_add(flags + FL_O + s * FS, 1u, RLX_AGENT);
        }
    }
}

constexpr int NREP = 3;
__device__ __forceinline__ void oproj_role(const ChainArgs& a, const int u) {
    const int n = u & 15, rp = u >> 4;
    const int tid = threadIdx.x, lane = tid & 63, w = __builtin_amdgcn_readfirstlane(tid >> 6);
    const int g = lane >> 4, c = lane & 15;
    const __amdgpu_buffer_rsrc_t ws_r = __builtin_amdgcn_make_buffer_rsrc(a.ws, 0, 0x7fffffff, 0x00020000);
    gu32* flags = (gu32*)(a.ws + WS_FLAG);
    gu32* tmo = flags + FL_TMO;
    u32x4 F[32][2];
    {
        const int kq = lane >> 4, gg = (lane >> 2) & 3, oct = lane & 3;
        const unsigned lb = (unsigned)w * 16384;
#pragma unroll
        for (int it = 0; it < 8; ++it) {
            const int kk = 32 * w + 4 * it + kq;
            const int hh = kk >> 2, eh = (kk >> 1) & 1, f = kk & 1;
            const bf16* Wp = a.Wo + (size_t)(hh * 128 + 32 * gg + 16 * f + 4 * eh) * 512 + 32 * n + 8 * oct;
            u32x4 r[8], fr[8];
#pragma unroll
            for (int j = 0; j < 8; ++j) r[j] = *(const u32x4*)(Wp + (size_t)(8 * (j >> 2) + (j & 3)) * 512);
            tr8x8(r, fr);
#pragma unroll
            for (int t = 0; t < 8; ++t) lds_wr16(lb + (unsigned)((((kq * 2 + (oct >> 1)) * 4 + gg) * 16 + 8 * (oct & 1) + t) * 16), fr[t]);
            asm volatile("s_waitcnt lgkmcnt(0)" ::: "memory");
#pragma unroll
            for (int q4 = 0; q4 < 4; ++q4)
#pragma unroll
                for (int T = 0; T < 2; ++T) { F[4 * it + q4][T] = lds_rd16(lb + (unsigned)(((q4 * 2 + T) * 64 + lane) * 16)); asm volatile("" : "+a"(F[4 * it + q4][T])); }
            asm volatile("s_waitcnt lgkmcnt(0)" ::: "memory");
        }
    }
    wg_barrier();
    for (int un = 0; un < 128; ++un) {
                if ((un & 3) != rp) continue;
        const int s = un >> 1, rh = un & 1;
        if (w == 0) {
            gu32* f = flags + FL_O + s * FS;
            for (unsigned spins = 0;; ++spins) {
                const unsigned v = __builtin_amdgcn_readfirstlane(__hip_atomic_load(f, RLX_AGENT));
                if (v == 31u) break;
                if (spins > SPIN_CAP) { __hip_atomic_store(tmo, 4u, RLX_AGENT); break; }
                __builtin_amdgcn_s_sleep(PSLEEP * 2);
            }
        }
        wg_barrier();
        const unsigned ab = (unsigned)(WS_OX + (size_t)((s * 2 + rh) * 128 + 32 * w) * 1024) + (unsigned)lane * 16;
        f32x4 d0 = {0, 0, 0, 0}, d1 = d0;
        asm volatile("s_nop 1" : "+v"(d0), "+v"(d1));
#pragma unroll
        for (int hb = 0; hb < 2; ++hb) {
            u32x4 A[16];
#pragma unroll
            for (int i = 0; i < 16; ++i) A[i] = __builtin_amdgcn_raw_buffer_load_b128(ws_r, ab + (unsigned)((16 * hb + i) * 1024), 0, 16);
#pragma unroll
            for (int i = 0; i < 16; ++i) { MFD(d0, A[i], F[16 * hb + i][0], "v", "a"); MFD(d1, A[i], F[16 * hb + i][1], "v", "a"); }
        }
        asm volatile("s_nop 7" : "+v"(d0), "+v"(d1));
        lds_wr16(65536u + (unsigned)(((w * 2 + 0) * 64 + lane) * 16), __builtin_bit_cast(u32x4, d0));
        lds_wr16(65536u + (unsigned)(((w * 2 + 1) * 64 + lane) * 16), __builtin_bit_cast(u32x4, d1));
        wg_barrier();
        {
            const int tok = tid >> 4, cp = tid & 15, T = cp >> 3, c0 = (2 * cp) & 15;
            float v0 = 0, v1 = 0;
#pragma unroll
            for (int ww = 0; ww < 4; ++ww) {
                const unsigned ad = 65536u + (unsigned)((((ww * 2 + T) * 64) + (tok >> 2) * 16 + c0) * 16 + (tok & 3) * 4);
                v0 += __uint_as_float(lds_rd4(ad)); v1 += __uint_as_float(lds_rd4(ad + 16));
            }
            *(unsigned*)(a.out + (size_t)(s * 32 + rh * 16 + tok) * 1024 + 512 + 32 * n + 2 * cp) = pk2(v0, v1);
        }
        wg_barrier();
    }
}

__device__ __forceinline__ void wait_count(gu32* f, unsigned want, gu32* tmo) {
    for (unsigned spins = 0;; ++spins) {
        const unsigned v = __builtin_amdgcn_readfirstlane(__hip_atomic_load(f, RLX_AGENT));
        if (v == want) return;
        if (spins > SPIN_CAP) { __hip_atomic_store(tmo, 5u, RLX_AGENT); return; }
        __builtin_amdgcn_s_sleep(8);
    }
}
template <int NV>
__device__ __forceinline__ void a1_unit(const ChainArgs& a, const int j, const int rg) {
    const int tid = threadIdx.x, lane = tid & 63, w = __builtin_amdgcn_readfirstlane(tid >> 6);
    const int g = lane >> 4, c = lane & 15;
    const __amdgpu_buffer_rsrc_t ws_r = __builtin_amdgcn_make_buffer_rsrc(a.ws, 0, 0x7fffffff, 0x00020000);
    const __amdgpu_buffer_rsrc_t x_r = __builtin_amdgcn_make_buffer_rsrc((void*)a.ax, 0, 2048 * 512 * 2, 0x00020000);
    u32x4 F0[2][16], F1[2][16], F2[2][16];
    load_panel<1, 1>(a.awqkv + 384 * j, 1536, F0, w, lane);
    load_panel<1, 1>(a.awqkv + 384 * j + 128, 1536, F1, w, lane);
    load_panel<1, 0, 0, 1>(a.awqkv + 384 * j + 256, 1536, F2, w, lane);
    auto load_x = [&](int t32, int buf) {
#pragma unroll
        for (int r = 0; r < 8; ++r) {
            unsigned keep;
            const unsigned voff = (unsigned)(((t32 * 32 + 8 * w + r) * 512) * 2 + lane * 16), dst = h_x(buf) + (unsigned)(8 * w + r) * 1040;
            asm volatile("s_mov_b32 %0, m0\n\ts_mov_b32 m0, %3\n\ts_nop 0\n\tbuffer_load_dwordx4 %1, %2, 0 offen lds\n\ts_mov_b32 m0, %0" : "=&s"(keep) : "v"(voff), "s"(x_r), "s"(dst) : "memory");
        }
    };
    load_x(rg * 4, 0);
    for (int i = 0; i < 4; ++i) {
        asm volatile("s_waitcnt vmcnt(0)" ::: "memory");
        wg_barrier();
        if (i + 1 < 4) load_x(rg * 4 + i + 1, (i + 1) & 1);
        const unsigned xb = h_x(i & 1) + (unsigned)(c * 1040 + g * 16);
        f32x4 d[3][2][2];
#pragma unroll
        for (int p = 0; p < 3; ++p)
#pragma unroll
            for (int t = 0; t < 2; ++t) { d[p][t][0] = (f32x4){0, 0, 0, 0}; d[p][t][1] = d[p][t][0]; }
        asm volatile("s_nop 1" : "+v"(d[0][0][0]), "+v"(d[0][0][1]), "+v"(d[0][1][0]), "+v"(d[0][1][1]), "+v"(d[1][0][0]), "+v"(d[1][0][1]), "+v"(d[1][1][0]), "+v"(d[1][1][1]));
        asm volatile("s_nop 1" : "+v"(d[2][0][0]), "+v"(d[2][0][1]), "+v"(d[2][1][0]), "+v"(d[2][1][1]));
#pragma unroll
        for (int kk = 0; kk < 16; ++kk) {
            const u32x4 x0 = lds_rd16(xb + (unsigned)(kk * 64)), x1 = lds_rd16(xb + (unsigned)(16 * 1040 + kk * 64));
#pragma unroll
            for (int t = 0; t < 2; ++t) {
                if (NV >= 3) { MFD(d[0][t][0], x0, F0[t][kk], "v", "a"); MFD(d[0][t][1], x1, F0[t][kk], "v", "a"); MFD(d[1][t][0], x0, F1[t][kk], "v", "a"); MFD(d[1][t][1], x1, F1[t][kk], "v", "a"); }
                else { MFD(d[0][t][0], F0[t][kk], x0, "a", "v"); MFD(d[0][t][1], F0[t][kk], x1, "a", "v"); MFD(d[1][t][0], F1[t][kk], x0, "a", "v"); MFD(d[1][t][1], F1[t][kk], x1, "a", "v"); }
                if (NV >= 1) { MFD(d[2][t][0], x0, F2[t][kk], "v", "v"); MFD(d[2][t][1], x1, F2[t][kk], "v", "v"); }
                else { MFD(d[2][t][0], F2[t][kk], x0, "v", "v"); MFD(d[2][t][1], F2[t][kk], x1, "v", "v"); }
            }
        }
        asm volatile("s_nop 7" : "+v"(d[0][0][0]), "+v"(d[0][0][1]), "+v"(d[0][1][0]), "+v"(d[0][1][1]), "+v"(d[1][0][0]), "+v"(d[1][0][1]), "+v"(d[1][1][0]), "+v"(d[1][1][1]));
        asm volatile("" : "+v"(d[2][0][0]), "+v"(d[2][0][1]), "+v"(d[2][1][0]), "+v"(d[2][1][1]));
#pragma unroll
        for (int p = 0; p < 3; ++p)
#pragma unroll
            for (int t = 0; t < 2; ++t)
#pragma unroll
                for (int tt = 0; tt < 2; ++tt) {
                    const int hd = (384 * j + 128 * p) / 16 + 2 * w + t;
                    const int tile = (rg * 4 + i) * 2 + tt;
                    const u32x2 v = {pk2(d[p][t][tt][0], d[p][t][tt][1]), pk2(d[p][t][tt][2], d[p][t][tt][3])};
                    __builtin_amdgcn_raw_buffer_store_b64(v, ws_r, (unsigned)(WS_AQ + (size_t)(hd * 128 + tile) * 512) + (unsigned)lane * 8, 0, 16);
                }
    }
    asm volatile("s_waitcnt vmcnt(0)" ::: "memory");
    wg_barrier();
    if (tid == 0) __hip_atomic_fetch_add((gu32*)(a.ws + WS_FLAG) + FL_A1 + rg * FS, 1u, RLX_AGENT);
}

__device__ __forceinline__ void a2_unit(const ChainArgs& a, const int un) {
    const int rg64 = un >> 2, ho = un & 3;
    const int tid = threadIdx.x, lane = tid & 63, w = __builtin_amdgcn_readfirstlane(tid >> 6);
    const int g = lane >> 4, c = lane & 15;
    const __amdgpu_buffer_rsrc_t ws_r = __builtin_amdgcn_make_buffer_rsrc(a.ws, 0, 0x7fffffff, 0x00020000);
    gu32* flags = (gu32*)(a.ws + WS_FLAG);
    if (w == 0) {
        const int lo = (64 * rg64 - 256) < 0 ? 0 : (64 * rg64 - 256) / 128, hi = (64 * rg64 + 63) / 128;
        for (int r = lo; r <= hi; ++r) wait_count(flags + FL_A1 + r * FS, 3u, flags + FL_TMO);
    }
    wg_barrier();
    const float sc2 = 0.25f * 1.4426950408889634f;
    for (int it = 0; it < 8; ++it) {
        const int hd = 8 * ho + 2 * w + (it >> 2), qt = 4 * rg64 + (it & 3);
        const unsigned qb = (unsigned)(WS_AQ + (size_t)(hd * 128) * 512) + (unsigned)lane * 8;
        const unsigned kb = qb + 32u * 128u * 512u, vb = kb + 32u * 128u * 512u;
        const u32x2 qf = __builtin_amdgcn_raw_buffer_load_b64(ws_r, qb + (unsigned)qt * 512, 0, 16);
        u32x2 kf[17], vf[17];
#pragma unroll
        for (int i = 0; i < 17; ++i) {
            const int kt = qt - 16 + i, ktc = kt < 0 ? 0 : kt;
            kf[i] = __builtin_amdgcn_raw_buffer_load_b64(ws_r, kb + (unsigned)ktc * 512, 0, 16);
            vf[i] = __builtin_amdgcn_raw_buffer_load_b64(ws_r, vb + (unsigned)ktc * 512, 0, 16);
        }
        f32x4 S[17];
#pragma unroll
        for (int i = 0; i < 17; ++i) {
            f32x4 z = {0, 0, 0, 0};
            S[i] = __builtin_amdgcn_mfma_f32_16x16x16bf16_1k(__builtin_bit_cast(bf16x4, kf[i]), __builtin_bit_cast(bf16x4, qf), z, 0, 0, 0);
        }
        const float NEG = -3.0e38f;
        float m = NEG;
#pragma unroll
        for (int i = 0; i < 17; ++i) {
            const int kt = qt - 16 + i;
#pragma unroll
            for (int r = 0; r < 4; ++r) {
                bool ok = kt >= 0;
                if (i == 0) ok = ok && (4 * g + r) > c;
                if (i == 16) ok = (4 * g + r) <= c;
                S[i][r] = ok ? S[i][r] : NEG;
                m = fmaxf(m, S[i][r]);
            }
        }
        m = fmaxf(m, xor16f(m)); m = fmaxf(m, xor32f(m));
        const float nm = -m * sc2;
        float l = 0;
        u32x2 P[18];
#pragma unroll
        for (int i = 0; i < 17; ++i) {
            float p[4];
#pragma unroll
            for (int r = 0; r < 4; ++r) { p[r] = S[i][r] == NEG ? 0.f : __builtin_amdgcn_exp2f(__builtin_fmaf(S[i][r], sc2, nm)); l += p[r]; }
            P[i] = (u32x2){pk2(p[0], p[1]), pk2(p[2], p[3])};
        }
        P[17] = (u32x2){0u, 0u};
        l += xor16f(l); l += xor32f(l);
        f32x4 o = {0, 0, 0, 0};
#pragma unroll
        for (int i = 0; i < 9; ++i) {
            const u32x2 v1 = (2 * i + 1 < 17) ? vf[2 * i + 1 < 17 ? 2 * i + 1 : 0] : (u32x2){0u, 0u};
            const u32x4 va = {vf[2 * i].x, vf[2 * i].y, v1.x, v1.y};
            const u32x4 pbv = {P[2 * i].x, P[2 * i].y, P[2 * i + 1].x, P[2 * i + 1].y};
            o = __builtin_amdgcn_mfma_f32_16x16x32_bf16(__builtin_bit_cast(bf16x8, va), __builtin_bit_cast(bf16x8, pbv), o, 0, 0, 0);
        }
        const float rl = __builtin_amdgcn_rcpf(l);
        const u32x2 ov = {pk2(o[0] * rl, o[1] * rl), pk2(o[2] * rl, o[3] * rl)};
        __builtin_amdgcn_raw_buffer_store_b64(ov, ws_r, (unsigned)(WS_AO + (size_t)(qt * 16 + (hd >> 1)) * 1024) + (unsigned)(lane * 16 + (hd & 1) * 8), 0, 16);
    }
    asm volatile("s_waitcnt vmcnt(0)" ::: "memory");
    wg_barrier();
    if (tid == 0) __hip_atomic_fetch_add(flags + FL_A2 + rg64 * FS, 1u, RLX_AGENT);
}

__device__ __forceinline__ void a3_unit(const ChainArgs& a, const int un) {
    const int js = un & 1, rgp = un >> 1;
    const int tid = threadIdx.x, lane = tid & 63, w = __builtin_amdgcn_readfirstlane(tid >> 6);
    const int g = lane >> 4, c = lane & 15;
    const __amdgpu_buffer_rsrc_t ws_r = __builtin_amdgcn_make_buffer_rsrc(a.ws, 0, 0x7fffffff, 0x00020000);
    gu32* flags = (gu32*)(a.ws + WS_FLAG);
    u32x4 F0[2][16], F1[2][16];
    load_panel<1, 1, 1>(a.awo + 256 * js, 512, F0, w, lane);
    load_panel<1, 1, 1>(a.awo + 256 * js + 128, 512, F1, w, lane);
    if (w == 0) { wait_count(flags + FL_A2 + (2 * rgp) * FS, 3u, flags + FL_TMO); wait_count(flags + FL_A2 + (2 * rgp + 1) * FS, 3u, flags + FL_TMO); }
    wg_barrier();
    for (int i = 0; i < 8; ++i) {
        const int tile = rgp * 8 + i;
        u32x4 A[16];
#pragma unroll
        for (int kk = 0; kk < 16; ++kk) A[kk] = __builtin_amdgcn_raw_buffer_load_b128(ws_r, (unsigned)(WS_AO + (size_t)(tile * 16 + kk) * 1024) + (unsigned)lane * 16, 0, 16);
        f32x4 d[2][2];
        d[0][0] = (f32x4){0, 0, 0, 0}; d[0][1] = d[0][0]; d[1][0] = d[0][0]; d[1][1] = d[0][0];
        asm volatile("s_nop 1" : "+v"(d[0][0]), "+v"(d[0][1]), "+v"(d[1][0]), "+v"(d[1][1]));
#pragma unroll
        for (int kk = 0; kk < 16; ++kk)
#pragma unroll
            for (int t = 0; t < 2; ++t) { MFD(d[0][t], F0[t][kk], A[kk], "a", "v"); MFD(d[1][t], F1[t][kk], A[kk], "a", "v"); }
        asm volatile("s_nop 7" : "+v"(d[0][0]), "+v"(d[0][1]), "+v"(d[1][0]), "+v"(d[1][1]));
#pragma unroll
        for (int p = 0; p < 2; ++p)
#pragma unroll
            for (int t = 0; t < 2; ++t) {
                const u32x2 v = {pk2(d[p][t][0], d[p][t][1]), pk2(d[p][t][2], d[p][t][3])};
                *(u32x2*)(a.out + (size_t)(tile * 16 + c) * 1024 + 256 * js + 128 * p + 16 * (2 * w + t) + 4 * g) = v;
            }
    }
}

__device__ __forceinline__ void a_role(const ChainArgs& a, const int ua) {
    if (ua < 64) {
        const int j = ua & 3, rg = ua >> 2;
        if (j < 2) a1_unit<0>(a, j, rg); else if (j == 2) a1_unit<1>(a, j, rg); else a1_unit<3>(a, j, rg);
    }
    wg_barrier();
    for (int un = ua; un < 128; un += 64) a2_unit(a, un);
    wg_barrier();
    if (ua >= 32) a3_unit(a, ua - 32);
}

__global__ void __launch_bounds__(256, 1) fused_kernel(ChainArgs a) {
    extern __shared__ unsigned char smem[];
    const int b = blockIdx.x;
    const int x = (b & 7) ^ 4, slot = b >> 3;
    if (x < 4) {
        if (slot < 16) chain_role(a, x * 16 + slot);
    } else {
        if (slot < 16) head_role(a, (x - 4) * 16 + slot);
        else { a_role(a, (x - 4) * 16 + (slot - 16)); wg_barrier(); oproj_role(a, (x - 4) * 16 + (slot - 16)); }
    }
}

extern "C" void kernel_launch(void* const* d_in, const int* in_sizes, int n_in, void* d_out, int out_size, void* d_ws, size_t ws_size, hipStream_t stream) {
    ChainArgs ca;
    ca.x = (const bf16*)d_in[3]; ca.out = (bf16*)d_out;
    ca.ax = (const bf16*)d_in[0]; ca.awqkv = (const bf16*)d_in[1]; ca.awo = (const bf16*)d_in[2];
    ca.mem0 = (const bf16*)d_in[4];
    ca.Wq = (const bf16*)d_in[5]; ca.Wk = (const bf16*)d_in[6]; ca.Wv = (const bf16*)d_in[7]; ca.Wo = (const bf16*)d_in[8];
    ca.ws = (unsigned char*)d_ws;
    (void)hipMemsetAsync(d_ws, 0xFF, WS_INIT_BYTES, stream);
    (void)hipFuncSetAttribute((const void*)fused_kernel, hipFuncAttributeMaxDynamicSharedMemorySize, LDS_BYTES);
    fused_kernel<<<256, 256, LDS_BYTES, stream>>>(ca);
}
```
